```python
import torch

def _softmax(x, axis=-1):
    return torch.softmax(x, dim=axis)
def _gelu(x):
    return torch.nn.functional.gelu(x, approximate="tanh")
def _silu(x):
    return torch.nn.functional.silu(x)
def _sigmoid(x):
    return torch.sigmoid(x)
def _layernorm(x, axis=-1, eps=1e-5):
    m = x.mean(dim=axis, keepdim=True)
    v = x.var(dim=axis, keepdim=True, unbiased=False)
    return (x - m) / torch.sqrt(v + eps)
def _scatter_add(tgt, idx, src, axis=0):
    return tgt.clone().index_add_(axis, idx, src)
def _segment_sum(src, seg, n):
    out = torch.zeros((n,) + src.shape[1:], dtype=src.dtype)
    return out.index_add_(0, seg, src)
def _pad(x, pw):
    flat = tuple(v for pair in reversed(pw) for v in pair)
    return torch.nn.functional.pad(x, flat)

kernel_name = 'concat_heads__gqa_block_cached__mha_block_cached__near_ridge'

def setup_inputs():

    _w = (lambda a: (lambda t: t.to(torch.bfloat16) if t.is_floating_point() else t)(torch.as_tensor(a)))
    def _A_setup():
        B, Sq, D = 32, 32, 512
        Hq, Hkv, Sk, Dh = 32, 32, 32, 128
        assert Hq % Hkv == 0, (Hq, Hkv)
        return {
            "x": _w((lambda *s: torch.randn(*s, dtype=torch.bfloat16) * 0.8)(B, Sq, D)),
            "k_cache": _w((lambda *s: torch.randn(*s, dtype=torch.bfloat16) * 0.8)(B, Hkv, Sk, Dh)),
            "v_cache": _w((lambda *s: torch.randn(*s, dtype=torch.bfloat16) * 0.05)(B, Hkv, Sk, Dh)),
            "wq": _w((lambda *s: torch.randn(*s, dtype=torch.bfloat16) * 0.8)(D, Hq * Dh) * (D ** -0.5)),
            "wo": _w((lambda *s: torch.randn(*s, dtype=torch.bfloat16) * 0.05)(Hq * Dh, D) * ((Hq * Dh) ** -0.5)),
        }
    _A_in = _A_setup()

    def _B_setup():
        B, Sq, D = 32, 32, 512
        H, Sk, Dh = 32, 32, 128
        return {
            "x": _w((lambda *s: torch.randn(*s, dtype=torch.bfloat16) * 0.8)(B, Sq, D)),
            "k_cache": _w((lambda *s: torch.randn(*s, dtype=torch.bfloat16) * 0.8)(B, H, Sk, Dh)),
            "v_cache": _w((lambda *s: torch.randn(*s, dtype=torch.bfloat16) * 0.05)(B, H, Sk, Dh)),
            "wq": _w((lambda *s: torch.randn(*s, dtype=torch.bfloat16) * 0.8)(D, H * Dh) * (D ** -0.5)),
            "wo": _w((lambda *s: torch.randn(*s, dtype=torch.bfloat16) * 0.05)(H * Dh, D) * ((H * Dh) ** -0.5)),
        }
    _B_in = _B_setup()

    return {
        "A_x": _A_in["x"],
        "A_k_cache": _A_in["k_cache"],
        "A_v_cache": _A_in["v_cache"],
        "A_wq": _A_in["wq"],
        "A_wo": _A_in["wo"],
        "B_x": _B_in["x"],
        "B_k_cache": _B_in["k_cache"],
        "B_v_cache": _B_in["v_cache"],
        "B_wq": _B_in["wq"],
        "B_wo": _B_in["wo"],
    }

def reference(A_x, A_k_cache, A_v_cache, A_wq, A_wo, B_x, B_k_cache, B_v_cache, B_wq, B_wo):
    def _A_ref(x, k_cache, v_cache, wq, wo):
        B, Sq, D = x.shape
        Hq, Dh = 32, 128
        Hkv = k_cache.shape[1]
        rep = Hq // Hkv
        q = (lambda x, *d: x.permute(*d))((x @ wq).reshape(B, Sq, Hq, Dh), 0, 2, 1, 3)
        k = (lambda x, n, axis: torch.repeat_interleave(x, n, dim=axis))(k_cache, rep, axis=1)
        v = (lambda x, n, axis: torch.repeat_interleave(x, n, dim=axis))(v_cache, rep, axis=1)
        scores = torch.einsum("bhqd,bhkd->bhqk", q, k) * (Dh ** -0.5)
        p = _softmax(scores, axis=-1)
        o = torch.einsum("bhqk,bhkd->bhqd", p, v)
        return (lambda x, *d: x.permute(*d))(o, 0, 2, 1, 3).reshape(B, Sq, Hq * Dh) @ wo

    def _B_ref(x, k_cache, v_cache, wq, wo):
        B, Sq, D = x.shape
        H, Dh = 32, 128
        q = (lambda x, *d: x.permute(*d))((x @ wq).reshape(B, Sq, H, Dh), 0, 2, 1, 3)
        scores = torch.einsum("bhqd,bhkd->bhqk", q, k_cache) * (Dh ** -0.5)
        p = _softmax(scores, axis=-1)
        o = torch.einsum("bhqk,bhkd->bhqd", p, v_cache)
        return (lambda x, *d: x.permute(*d))(o, 0, 2, 1, 3).reshape(B, Sq, H * Dh) @ wo

    a = _A_ref(A_x, A_k_cache, A_v_cache, A_wq, A_wo)
    b = _B_ref(B_x, B_k_cache, B_v_cache, B_wq, B_wo)
    return torch.concatenate([a, b], axis=-1)
```

```cpp
#include <hip/hip_runtime.h>
#include <hip/hip_bf16.h>

typedef unsigned short u16;
typedef __attribute__((ext_vector_type(4))) short s16x4;
typedef __attribute__((ext_vector_type(8))) short s16x8;
typedef __attribute__((ext_vector_type(8))) __bf16 bf16x8;
typedef __attribute__((ext_vector_type(2))) __bf16 bf16x2;
typedef __attribute__((ext_vector_type(2))) float f32x2;
typedef __attribute__((ext_vector_type(4))) float f32x4;
typedef __attribute__((ext_vector_type(16))) float f32x16;
typedef __attribute__((ext_vector_type(4))) unsigned u32x4;
#define LDS_AS __attribute__((address_space(3)))
#define DI __device__ __forceinline__

#define WAIT_V(n) asm volatile("s_waitcnt vmcnt(" #n ")" ::: "memory")
#define BAR() do { asm volatile("s_waitcnt lgkmcnt(0)" ::: "memory"); __builtin_amdgcn_s_barrier(); } while (0)

DI bf16x8 as_bf(s16x8 v) { return __builtin_bit_cast(bf16x8, v); }
DI bf16x8 as_bf(u32x4 v) { return __builtin_bit_cast(bf16x8, v); }
DI unsigned pk2(float a, float b) { bf16x2 r = __builtin_convertvector(f32x2{a, b}, bf16x2); return __builtin_bit_cast(unsigned, r); }
DI float bfr(float a) { bf16x2 r = __builtin_convertvector(f32x2{a, a}, bf16x2); return __uint_as_float(__builtin_bit_cast(unsigned, r) << 16); }
DI f32x16 mfma(bf16x8 a, bf16x8 b, f32x16 c) { return __builtin_amdgcn_mfma_f32_32x32x16_bf16(a, b, c, 0, 0, 0); }
typedef LDS_AS char* lptr;
DI s16x4 trread(lptr addr) { return __builtin_amdgcn_ds_read_tr16_b64_v4i16((LDS_AS s16x4*)addr); }
DI bf16x8 trfrag(lptr addr, int off0, int off1) {
  s16x4 lo = trread(addr + off0), hi = trread(addr + off1);
  return as_bf(__builtin_shufflevector(lo, hi, 0, 1, 2, 3, 4, 5, 6, 7));
}
DI bf16x8 ldsfrag(lptr addr) { return *(LDS_AS bf16x8*)addr; }
DI void glds16(unsigned voff, const void* sbase, lptr lds_addr) {
  unsigned keep; const unsigned dst = (unsigned)(size_t)lds_addr;
  asm volatile("s_mov_b32 %0, m0\n\ts_mov_b32 m0, %2\n\ts_nop 0\n\tglobal_load_lds_dwordx4 %1, %3\n\ts_mov_b32 m0, %0"
               : "=&s"(keep) : "v"(voff), "s"(dst), "s"(sbase) : "memory");
}
DI u32x4 pack8(const f32x16& a, int s) {
  u32x4 r;
  r[0] = pk2(a[8 * s + 0], a[8 * s + 1]); r[1] = pk2(a[8 * s + 2], a[8 * s + 3]);
  r[2] = pk2(a[8 * s + 4], a[8 * s + 5]); r[3] = pk2(a[8 * s + 6], a[8 * s + 7]);
  return r;
}

constexpr int K1_STAGE = 49152;
constexpr int K1_LDS = 3 * K1_STAGE;

__global__ __launch_bounds__(512, 2) void k1_qattn(const u16* __restrict__ xA, const u16* __restrict__ kA, const u16* __restrict__ vA, const u16* __restrict__ wqA,
                                                   const u16* __restrict__ xB, const u16* __restrict__ kB, const u16* __restrict__ vB, const u16* __restrict__ wqB,
                                                   u16* __restrict__ o_ws) {
  extern __shared__ __attribute__((aligned(16))) char smem[];
  const lptr lds0 = (lptr)smem;
  const int id = blockIdx.x, g = id & 7, j = id >> 3;
  const int br = g >> 2, ho = g & 3, rg = j >> 2, hp = j & 3;
  const int tid = threadIdx.x, lane = tid & 63;
  const int w = __builtin_amdgcn_readfirstlane(tid >> 6);
  const int bi = w >> 1, hi = w & 1;
  const u16* x = br ? xB : xA; const u16* kc = br ? kB : kA; const u16* vc = br ? vB : vA; const u16* wq = br ? wqB : wqA;
  const int head0 = ho * 8 + hp * 2;
  const int b = rg * 4 + bi, head = head0 + hi;

  unsigned xsrc[2], wsrc[4];
#pragma unroll
  for (int i = 0; i < 2; ++i) {
    const int row = (2 * w + i) * 8 + (lane >> 3), c = (lane & 7) ^ ((row >> 1) & 7);
    xsrc[i] = (row * 512 + 8 * c) * 2;
  }
#pragma unroll
  for (int i = 0; i < 4; ++i) {
    const int kr = (4 * w + i) * 2 + (lane >> 5), c = (lane & 31) ^ ((kr & 3) << 2);
    wsrc[i] = (kr * 4096 + 8 * c) * 2;
  }
  const u16* xb0 = x + (size_t)rg * 128 * 512;
  const u16* wb0 = wq + head0 * 128;
  auto stage = [&](int s) {
    const lptr base = lds0 + (s % 3) * K1_STAGE;
#pragma unroll
    for (int i = 0; i < 2; ++i) glds16(xsrc[i], xb0 + 64 * s, base + (2 * w + i) * 1024);
#pragma unroll
    for (int i = 0; i < 4; ++i) glds16(wsrc[i], wb0 + (size_t)64 * s * 4096, base + 16384 + (4 * w + i) * 1024);
  };
  stage(0); stage(1); stage(2);
  const int r32 = lane & 31, h = lane >> 5;
  const u16* kbase = kc + (size_t)(b * 32 + head) * 4096;
  const u16* vbase = vc + (size_t)(b * 32 + head) * 4096;
  bf16x8 kf[8]; u32x4 vr[8];
#pragma unroll
  for (int f = 0; f < 8; ++f) kf[f] = *(const bf16x8*)(kbase + r32 * 128 + 16 * f + 8 * h);
#pragma unroll
  for (int i = 0; i < 8; ++i) vr[i] = *(const u32x4*)(vbase + i * 512 + lane * 8);

  const int q4 = (lane & 15) >> 2, p4 = lane & 3, g16 = (lane >> 4) & 1, u4 = ((p4 & 1) << 1) | (p4 >> 1);
  const int xrow = bi * 32 + r32;
  const unsigned xoff = xrow * 128 + ((h ^ ((xrow >> 1) & 7)) << 4);
  const unsigned woff = 16384 + (8 * h + q4) * 512 + (4 * hi + q4) * 64 + g16 * 32 + u4 * 8;

  f32x16 acc[4];
#pragma unroll
  for (int t = 0; t < 4; ++t) acc[t] = f32x16{};

#pragma unroll
  for (int s = 0; s < 8; ++s) {
    if (s == 0) WAIT_V(28); else if (s <= 2) WAIT_V(22); else if (s < 7) WAIT_V(6); else WAIT_V(0);
    BAR();
    if (s >= 1 && s + 2 < 8) stage(s + 2);
    if (s == 7) {
      const lptr vl = lds0 + (w >> 2) * 2 * K1_STAGE + (w & 3) * 8192;
#pragma unroll
      for (int i = 0; i < 8; ++i) {
        const int key = 4 * i + (lane >> 4), c = (lane & 15) ^ ((key & 3) << 2);
        *(LDS_AS u32x4*)(vl + key * 256 + c * 16) = vr[i];
      }
    }
    const lptr base = lds0 + (s % 3) * K1_STAGE;
    bf16x8 xb[2], wa[2][4];
    xb[0] = ldsfrag(base + xoff);
#pragma unroll
    for (int t = 0; t < 4; ++t) wa[0][t] = trfrag(base + (woff ^ (t << 6)), 0, 2048);
#pragma unroll
    for (int ks = 0; ks < 4; ++ks) {
      if (ks < 3) {
        xb[(ks + 1) & 1] = ldsfrag(base + (xoff ^ ((ks + 1) << 5)));
#pragma unroll
        for (int t = 0; t < 4; ++t) wa[(ks + 1) & 1][t] = trfrag(base + (woff ^ (t << 6)), (ks + 1) * 8192, (ks + 1) * 8192 + 2048);
      }
#pragma unroll
      for (int t = 0; t < 4; ++t) acc[t] = mfma(wa[ks & 1][t], xb[ks & 1], acc[t]);
      __builtin_amdgcn_sched_barrier(0);
    }
  }
  f32x16 sc = f32x16{};
#pragma unroll
  for (int t = 0; t < 4; ++t)
#pragma unroll
    for (int s2 = 0; s2 < 2; ++s2) sc = mfma(kf[2 * t + s2], as_bf(pack8(acc[t], s2)), sc);
  constexpr float SCALE = 0.08838834764831845f, LOG2E = 1.4426950408889634f;
  float m = -3.0e38f;
#pragma unroll
  for (int i = 0; i < 16; ++i) { sc[i] = bfr(bfr(sc[i]) * SCALE); m = fmaxf(m, sc[i]); }
  m = fmaxf(m, __shfl_xor(m, 32));
  float sum = 0.f;
#pragma unroll
  for (int i = 0; i < 16; ++i) { sc[i] = __builtin_amdgcn_exp2f((sc[i] - m) * LOG2E); sum += sc[i]; }
  sum += __shfl_xor(sum, 32);
  const float inv = 1.0f / sum;
#pragma unroll
  for (int i = 0; i < 16; ++i) sc[i] *= inv;
  const bf16x8 pf0 = as_bf(pack8(sc, 0)), pf1 = as_bf(pack8(sc, 1));
  const lptr vl = lds0 + (w >> 2) * 2 * K1_STAGE + (w & 3) * 8192;
  const unsigned voff = (4 * h + q4) * 256 + q4 * 64 + g16 * 32 + u4 * 8;
  u16* odst = o_ws + ((size_t)((br * 32 + b) * 32 + head) * 8 * 64 + lane) * 8;
#pragma unroll
  for (int t = 0; t < 4; ++t) {
    f32x16 o = f32x16{};
    o = mfma(trfrag(vl + (voff ^ (t << 6)), 0, 2048), pf0, o);
    o = mfma(trfrag(vl + (voff ^ (t << 6)), 4096, 4096 + 2048), pf1, o);
    *(u32x4*)(odst + (2 * t) * 512) = pack8(o, 0);
    *(u32x4*)(odst + (2 * t + 1) * 512) = pack8(o, 1);
  }
}

constexpr int K2_STAGE = 32768;
constexpr int K2_NST = 4;
constexpr int K2_LDS = K2_NST * K2_STAGE;

__global__ __launch_bounds__(512, 2) void k2_oproj(const u16* __restrict__ woA, const u16* __restrict__ woB, const u16* __restrict__ o_ws, float* __restrict__ part) {
  extern __shared__ __attribute__((aligned(16))) char smem[];
  const lptr lds0 = (lptr)smem;
  const int id = blockIdx.x, g = id & 7, j = id >> 3;
  const int br = g >> 2, ho = g & 3, rg = j >> 2, np = j & 3;
  const int tid = threadIdx.x, lane = tid & 63;
  const int w = __builtin_amdgcn_readfirstlane(tid >> 6);
  const int kh = w >> 2, wn = (w >> 1) & 1, wr = w & 1, w4 = w & 3;
  const u16* wo = br ? woB : woA;
  unsigned wsrc[2];
#pragma unroll
  for (int i = 0; i < 2; ++i) {
    const int kr = (2 * w4 + i) * 4 + (lane >> 4), c = (lane & 15) ^ ((kr & 3) << 2);
    wsrc[i] = ((kh * 512 + kr) * 512 + 8 * c) * 2;
  }
  const u16* wb0 = wo + (size_t)(ho * 1024) * 512 + np * 128;
  const unsigned osrc = (((w4 * 32 + kh * 4) * 8 * 64 + lane) * 8) * 2;
  const u16* ob0 = o_ws + (size_t)((br * 32 + rg * 4) * 32 + ho * 8) * 8 * 64 * 8;
  auto stage = [&](int s) {
    const lptr base = lds0 + (s % K2_NST) * K2_STAGE + kh * 16384;
#pragma unroll
    for (int i = 0; i < 2; ++i) glds16(wsrc[i], wb0 + (size_t)32 * s * 512, base + (2 * w4 + i) * 1024);
#pragma unroll
    for (int i = 0; i < 2; ++i) glds16(osrc, ob0 + s * 1024 + i * 512, base + 8192 + (2 * w4 + i) * 1024);
  };
  stage(0); stage(1); stage(2);
  const int h = lane >> 5;
  const int q4 = (lane & 15) >> 2, p4 = lane & 3, g16 = (lane >> 4) & 1, u4 = ((p4 & 1) << 1) | (p4 >> 1);
  const unsigned woff = kh * 16384 + (8 * h + q4) * 256 + ((2 * wn) ^ q4) * 64 + g16 * 32 + u4 * 8;
  const unsigned ooff = kh * 16384 + 8192 + (wr * 2) * 2048 + lane * 16;
  f32x16 acc[2][2];
#pragma unroll
  for (int a = 0; a < 2; ++a)
#pragma unroll
    for (int c = 0; c < 2; ++c) acc[a][c] = f32x16{};
#pragma unroll
  for (int s = 0; s < 16; ++s) {
    if (s < 14) WAIT_V(8); else if (s == 14) WAIT_V(4); else WAIT_V(0);
    BAR();
    if (s + 3 < 16) stage(s + 3);
    const lptr base = lds0 + (s % K2_NST) * K2_STAGE;
#pragma unroll
    for (int ks = 0; ks < 2; ++ks) {
      bf16x8 wa[2], ob[2];
#pragma unroll
      for (int t = 0; t < 2; ++t) wa[t] = trfrag(base + (woff ^ (t << 6)), ks * 4096, ks * 4096 + 1024);
#pragma unroll
      for (int t = 0; t < 2; ++t) ob[t] = ldsfrag(base + ooff + t * 2048 + ks * 1024);
#pragma unroll
      for (int a = 0; a < 2; ++a)
#pragma unroll
        for (int c = 0; c < 2; ++c) acc[a][c] = mfma(wa[a], ob[c], acc[a][c]);
    }
  }
  {
    const lptr my = lds0 + w * 8192 + lane * 16, other = lds0 + (w ^ 4) * 8192 + lane * 16;
#pragma unroll
    for (int c = 0; c < 2; ++c)
#pragma unroll
      for (int q = 0; q < 4; ++q) {
        f32x4 v;
#pragma unroll
        for (int e = 0; e < 4; ++e) v[e] = kh ? acc[0][c][4 * q + e] : acc[1][c][4 * q + e];
        *(LDS_AS f32x4*)(my + (c * 4 + q) * 1024) = v;
      }
    BAR();
    float* dst = part + ((size_t)(((((ho * 2 + br) * 8 + rg) * 4 + np) * 2 + wn) * 2 + kh) * 4 + wr * 2) * 1024 + lane * 4;
#pragma unroll
    for (int c = 0; c < 2; ++c)
#pragma unroll
      for (int q = 0; q < 4; ++q) {
        f32x4 v = *(LDS_AS f32x4*)(other + (c * 4 + q) * 1024);
#pragma unroll
        for (int e = 0; e < 4; ++e) v[e] += kh ? acc[1][c][4 * q + e] : acc[0][c][4 * q + e];
        *(f32x4*)(dst + c * 1024 + q * 256) = v;
      }
  }
}

__global__ __launch_bounds__(512) void k3_reduce(const float* __restrict__ part, u16* __restrict__ out) {
  const int item = blockIdx.x * 8 + (threadIdx.x >> 6), lane = threadIdx.x & 63;
  const int tile = item >> 1, s2 = item & 1;
  const int tr = tile & 1, wr = (tile >> 1) & 1, tn = (tile >> 2) & 1, wn = (tile >> 3) & 1, np = (tile >> 4) & 3, rg = (tile >> 6) & 7, br = tile >> 9;
  const float* src = part + (size_t)tile * 1024 + s2 * 512 + lane * 4;
  f32x4 a[4], c[4];
#pragma unroll
  for (int ho = 0; ho < 4; ++ho) { a[ho] = *(const f32x4*)(src + (size_t)ho * 1048576); c[ho] = *(const f32x4*)(src + (size_t)ho * 1048576 + 256); }
  f32x4 s0 = (a[0] + a[1]) + (a[2] + a[3]), s1 = (c[0] + c[1]) + (c[2] + c[3]);
  u32x4 r; r[0] = pk2(s0[0], s0[1]); r[1] = pk2(s0[2], s0[3]); r[2] = pk2(s1[0], s1[1]); r[3] = pk2(s1[2], s1[3]);
  const int row = rg * 128 + wr * 64 + tr * 32 + (lane & 31);
  const int col = br * 512 + np * 128 + wn * 64 + tn * 32 + 16 * s2 + 8 * (lane >> 5);
  *(u32x4*)(out + (size_t)row * 1024 + col) = r;
}

extern "C" void kernel_launch(void* const* d_in, const int* in_sizes, int n_in, void* d_out, int out_size, void* d_ws, size_t ws_size, hipStream_t stream) {
  const u16* xA = (const u16*)d_in[0]; const u16* kA = (const u16*)d_in[1]; const u16* vA = (const u16*)d_in[2]; const u16* wqA = (const u16*)d_in[3]; const u16* woA = (const u16*)d_in[4];
  const u16* xB = (const u16*)d_in[5]; const u16* kB = (const u16*)d_in[6]; const u16* vB = (const u16*)d_in[7]; const u16* wqB = (const u16*)d_in[8]; const u16* woB = (const u16*)d_in[9];
  u16* o_ws = (u16*)d_ws;
  float* part = (float*)((char*)d_ws + (16u << 20));
  hipFuncSetAttribute((const void*)k1_qattn, hipFuncAttributeMaxDynamicSharedMemorySize, K1_LDS);
  hipFuncSetAttribute((const void*)k2_oproj, hipFuncAttributeMaxDynamicSharedMemorySize, K2_LDS);
  k1_qattn<<<256, 512, K1_LDS, stream>>>(xA, kA, vA, wqA, xB, kB, vB, wqB, o_ws);
  k2_oproj<<<256, 512, K2_LDS, stream>>>(woA, woB, o_ws, part);
  k3_reduce<<<256, 512, 0, stream>>>(part, (u16*)d_out);
}
```

```cpp
#include <hip/hip_runtime.h>
#include <hip/hip_bf16.h>
#include <utility>

typedef unsigned short u16;
typedef unsigned long long u64;
typedef __attribute__((ext_vector_type(4))) short s16x4;
typedef __attribute__((ext_vector_type(8))) short s16x8;
typedef __attribute__((ext_vector_type(8))) __bf16 bf16x8;
typedef __attribute__((ext_vector_type(2))) __bf16 bf16x2;
typedef __attribute__((ext_vector_type(2))) float f32x2;
typedef __attribute__((ext_vector_type(4))) float f32x4;
typedef __attribute__((ext_vector_type(16))) float f32x16;
typedef __attribute__((ext_vector_type(4))) unsigned u32x4;
#define LDS_AS __attribute__((address_space(3)))
#define GAS __attribute__((address_space(1)))
#define DI __device__ __forceinline__
typedef LDS_AS char* lptr;
typedef GAS u64 gu64;
#define RLX_AGENT __ATOMIC_RELAXED, __HIP_MEMORY_SCOPE_AGENT
#define DRAIN0 0
#define WOW_U 10
template <int N> using IC = std::integral_constant<int, N>;

template <int N> DI void wait_v() { asm volatile("s_waitcnt vmcnt(%0)" ::"n"(N) : "memory"); }
#define SB() __builtin_amdgcn_sched_barrier(0)
#define BARRIER() asm volatile("s_barrier" ::: "memory")

DI bf16x8 as_bf(s16x8 v) { return __builtin_bit_cast(bf16x8, v); }
DI bf16x8 as_bf(u32x4 v) { return __builtin_bit_cast(bf16x8, v); }
DI unsigned pk2(float a, float b) { bf16x2 r = __builtin_convertvector(f32x2{a, b}, bf16x2); return __builtin_bit_cast(unsigned, r); }
DI float bfr(float a) { bf16x2 r = __builtin_convertvector(f32x2{a, a}, bf16x2); return __uint_as_float(__builtin_bit_cast(unsigned, r) << 16); }
DI f32x16 mfma(bf16x8 a, bf16x8 b, f32x16 c) { return __builtin_amdgcn_mfma_f32_32x32x16_bf16(a, b, c, 0, 0, 0); }
DI s16x4 trread(lptr addr) { return __builtin_amdgcn_ds_read_tr16_b64_v4i16((LDS_AS s16x4*)addr); }
DI bf16x8 trfrag(lptr addr, int off0, int off1) {
  s16x4 lo = trread(addr + off0), hi = trread(addr + off1);
  return as_bf(__builtin_shufflevector(lo, hi, 0, 1, 2, 3, 4, 5, 6, 7));
}
DI bf16x8 ldsfrag(lptr addr) { return *(LDS_AS bf16x8*)addr; }
#define POL_X 0
#define POL_WQ 0
#define POL_WO 0
template <int POL> DI void glds16(unsigned voff, const void* sbase, lptr lds_addr) {
  unsigned keep; const unsigned dst = (unsigned)(size_t)lds_addr;
  if constexpr (POL == 0)
    asm volatile("s_mov_b32 %0, m0\n\ts_mov_b32 m0, %2\n\ts_nop 0\n\tglobal_load_lds_dwordx4 %1, %3\n\ts_mov_b32 m0, %0"
                 : "=&s"(keep) : "v"(voff), "s"(dst), "s"(sbase) : "memory");
  else if constexpr (POL == 1)
    asm volatile("s_mov_b32 %0, m0\n\ts_mov_b32 m0, %2\n\ts_nop 0\n\tglobal_load_lds_dwordx4 %1, %3 nt\n\ts_mov_b32 m0, %0"
                 : "=&s"(keep) : "v"(voff), "s"(dst), "s"(sbase) : "memory");
  else
    asm volatile("s_mov_b32 %0, m0\n\ts_mov_b32 m0, %2\n\ts_nop 0\n\tglobal_load_lds_dwordx4 %1, %3 sc1\n\ts_mov_b32 m0, %0"
                 : "=&s"(keep) : "v"(voff), "s"(dst), "s"(sbase) : "memory");
}
DI u32x4 pack8(const f32x16& a, int s) {
  u32x4 r;
  r[0] = pk2(a[8 * s + 0], a[8 * s + 1]); r[1] = pk2(a[8 * s + 2], a[8 * s + 3]);
  r[2] = pk2(a[8 * s + 4], a[8 * s + 5]); r[3] = pk2(a[8 * s + 6], a[8 * s + 7]);
  return r;
}
template <int... Is, class F> DI void sfor_impl(std::integer_sequence<int, Is...>, F&& f) { (f(std::integral_constant<int, Is>{}), ...); }
template <int N, class F> DI void sfor(F&& f) { sfor_impl(std::make_integer_sequence<int, N>{}, f); }

#define ST_DECL(n)
#define ST(i)
#define ST_OUT(ws, n)

struct Op { int key, tag, piece; };
enum { TU = 100, TK = 200, TV = 220, TWARM = 300, TST = 400, TWU = 500, TOU = 600 };
struct Sched { Op op[256]; int n; };
constexpr Sched makeS(int grp) {
  Sched s{}; int n = 0;
  auto add = [&](int key, int tag, int piece) { s.op[n++] = Op{key, tag, piece}; };
  if (grp == 0) {
    for (int i = 0; i < 8; ++i) add(0, TU + 0, i);
    for (int i = 0; i < 8; ++i) add(1, TU + 1, i);
  } else {
    for (int i = 0; i < 16; ++i) add(2, TWARM, i);
  }
  for (int u = 0; u < 16; ++u) {
    const int k0 = 100 + u * 10;
    if (grp == 0) {
      if (u + 2 < 16) for (int i = 0; i < 4; ++i) { add(k0 + i, TU + u + 2, i); add(k0 + i, TU + u + 2, 4 + i); }
    } else {
      if (u < 2) for (int i = 0; i < 4; ++i) add(k0 + i, TK + 0, 4 * u + i);
      if (u == 2 || u == 3) for (int i = 0; i < 4; ++i) add(k0 + i, TV + 0, 4 * (u - 2) + i);
      if (u == 4 || u == 5) for (int i = 0; i < 4; ++i) add(k0 + i, TK + 1, 4 * (u - 4) + i);
      if (u == 6 || u == 7) for (int i = 0; i < 4; ++i) add(k0 + i, TV + 1, 4 * (u - 6) + i);
    }
    if (u == WOW_U) add(k0 + 1, TWARM + 1, 0);
    if (u == 14) { add(k0 + 0, TWU + 0, 0); add(k0 + 1, TWU + 0, 1); add(k0 + 2, TOU + 0, 0); }
    if (u == 15) { add(k0 + 0, TWU + 1, 0); add(k0 + 1, TWU + 1, 1); add(k0 + 2, TOU + 1, 0); }
    if (u == 7) for (int i = 0; i < 4; ++i) add(k0 + 9, TST + 0, i);
    if (u == 15) { add(k0 + 8, TWU + 2, 0); add(k0 + 8, TWU + 2, 1); add(k0 + 8, TOU + 2, 0); for (int i = 0; i < 4; ++i) add(k0 + 9, TST + 1, i); }
  }
  auto add2 = [&](int v, int p, int tag) { add(300 + v * 10 + p, tag, 0); add(300 + v * 10 + p + 1, tag, 1); };
  add2(0, 0, TWU + 3); add(300 + 2, TOU + 3, 0);
  add2(1, 0, TWU + 4); add2(1, 2, TOU + 4);
  add2(2, 0, TWU + 5); add2(2, 2, TOU + 5);
  add2(3, 0, TWU + 6); add2(3, 2, TOU + 6);
  add2(4, 0, TWU + 7); add2(4, 1, TOU + 7); add2(4, 2, TWU + 8);
  add2(5, 0, TOU + 8); add2(5, 1, TWU + 9); add2(5, 2, TOU + 9);
  for (int v = 6; v <= 11; ++v) { add2(v, 0, TWU + v + 4); add2(v, 2, TOU + v + 4); }
  for (int i = 1; i < n; ++i) { Op o = s.op[i]; int j = i - 1; while (j >= 0 && s.op[j].key > o.key) { s.op[j + 1] = s.op[j]; --j; } s.op[j + 1] = o; }
  s.n = n; return s;
}
constexpr Sched SG[2] = {makeS(0), makeS(1)};
constexpr int swait(int grp, int key, int a, int b = -1, int c = -1) {
  const Sched& S = SG[grp];
  int issued = 0, last = -1;
  for (int i = 0; i < S.n; ++i) if (S.op[i].key < key) { const int t = S.op[i].tag; if (t == a || t == b || t == c) last = i; issued = i + 1; }
  if (last < 0) return 63;
  const int r = issued - 1 - last; return r > 63 ? 63 : r;
}
constexpr int w1wait(int grp, int u) {
  const int k0 = 100 + u * 10;
  if (grp == 0) return swait(0, k0, TU + u, u == 10 ? TST : -1);
  if (u == 2) return swait(1, k0, TWARM);
  if (u == 7) return swait(1, k0, TK, TV);
  if (u == 10) return swait(1, k0, TST);
  if (u == 15) return swait(1, k0, TWARM + 1);
  return 63;
}
constexpr int WSL[16] = {2, 4, 0, 6, 2, 4, 0, 6, 8, 2, 4, 0, 6, 8, 2, 4};
constexpr int OSL[16] = {3, 5, 1, 7, 3, 5, 1, 7, 9, 3, 5, 1, 7, 9, 3, 5};

constexpr int LDS_BYTES = 163840, KBLK = 98304, VBLK = 131072;
constexpr size_t WS_SLAB = 16u << 20, WS_SYNC = 32u << 20;

DI void poll3(gu64* f0, u64 tag, int lane, int skip) {
  for (unsigned spins = 0; spins < (1u << 22); ++spins) {
    u64 v = tag;
    if (lane < 4 && lane != skip) v = __hip_atomic_load(f0 + lane * 16, RLX_AGENT);
    if (__all(v == tag)) return;
    __builtin_amdgcn_s_sleep(1);
  }
}

__global__ __launch_bounds__(512, 2) void fused(const u16* __restrict__ xA, const u16* __restrict__ kA, const u16* __restrict__ vA, const u16* __restrict__ wqA, const u16* __restrict__ woA,
                                                const u16* __restrict__ xB, const u16* __restrict__ kB, const u16* __restrict__ vB, const u16* __restrict__ wqB, const u16* __restrict__ woB,
                                                char* __restrict__ ws, u16* __restrict__ out) {
  extern __shared__ __attribute__((aligned(16))) char smem[];
  const lptr lds0 = (lptr)smem;
  const int id = blockIdx.x, g = id & 7, j = id >> 3;
  const int br = g >> 2, ho = g & 3, rg = j >> 2, hp = j & 3;
  const int tid = threadIdx.x, lane = tid & 63;
  const int w = __builtin_amdgcn_readfirstlane(tid >> 6);
  const u16* x = br ? xB : xA; const u16* kc = br ? kB : kA; const u16* vc = br ? vB : vA; const u16* wq = br ? wqB : wqA; const u16* wo = br ? woB : woA;
  ST_DECL(48); ST(0);
  gu64* sy1 = (gu64*)(ws + WS_SYNC) + (size_t)((br * 4 + ho) * 8 + rg) * 256;
  gu64* sy3 = (gu64*)(ws + WS_SYNC) + (size_t)(64 + (br * 8 + rg) * 4 + hp) * 256;
  u64 ep1, ep3;
  asm volatile("s_load_dwordx2 %0, %2, 0x0\n\ts_load_dwordx2 %1, %3, 0x0" : "=&s"(ep1), "=&s"(ep3) : "s"(sy1), "s"(sy3) : "memory");

  const int r32 = lane & 31, h = lane >> 5;
  const int q4 = (lane & 15) >> 2, p4 = lane & 3, g16 = (lane >> 4) & 1, u4 = ((p4 & 1) << 1) | (p4 >> 1);
  const int head0 = ho * 8 + hp * 2;
  const int bi = w >> 1, dh = w & 1;
  const int kh = w >> 2, wn = (w >> 1) & 1, wr = w & 1, w4 = w & 3;
  const int b = rg * 4 + bi;

  unsigned srcA[4], srcB[4], wusrc[2];
#pragma unroll
  for (int i = 0; i < 4; ++i) {
    const int row = (4 * w4 + i) * 8 + (lane >> 3), c = (lane & 7) ^ ((row >> 1) & 7);
    const int kr = (4 * w4 + i) * 4 + (lane >> 4), c2 = (lane & 15) ^ ((kr & 3) << 2);
    srcA[i] = (row * 512 + 8 * c) * 2;
    srcB[i] = (kr * 4096 + 8 * c2) * 2;
  }
#pragma unroll
  for (int i = 0; i < 2; ++i) {
    const int kr2 = (2 * w4 + i) * 4 + (lane >> 4), c2 = (lane & 15) ^ ((kr2 & 3) << 2);
    wusrc[i] = (kr2 * 512 + 8 * c2) * 2;
  }
  const unsigned ksrc = ((lane >> 3) * 128 + 8 * ((lane & 7) ^ ((lane >> 4) & 3))) * 2;
  const unsigned vsrc = ((lane >> 3) * 128 + 8 * ((lane & 7) ^ (((lane >> 4) & 1) << 2))) * 2;
  const unsigned warm4 = (((w4 * 32 + (lane >> 4)) * 512) + (lane & 15) * 8) * 2;
  const unsigned warmq = ((w4 * 16 + (lane >> 5)) * 4096 + (lane & 31) * 8) * 2;
  const u16* xb0 = x + (size_t)rg * 128 * 512;
  const u16* wb0 = wq + head0 * 128;
  const u16* kb0 = kc + (size_t)((rg * 4 + w4) * 32 + head0) * 4096;
  const u16* vb0 = vc + (size_t)((rg * 4 + w4) * 32 + head0) * 4096;
  const u16* wub = wo + (size_t)(ho * 1024) * 512 + hp * 128;
  const u16* ogb = (const u16*)ws + (size_t)((br * 32 + rg * 4) * 32 + ho * 8) * 4096;
  const lptr kreg = lds0 + KBLK + w * 4096, vreg = lds0 + VBLK + w * 4096;
  int hl[4];
  hl[0] = 2 * hp + kh;
  hl[1] = 2 * ((hp + (kh ? 3 : 1)) & 3);
  hl[2] = 2 * ((hp + 2) & 3) + kh;
  hl[3] = 2 * ((hp + (kh ? 3 : 1)) & 3) + 1;
  const unsigned ousrc = ((w4 * 32) * 4096 + lane * 8) * 2;
  const unsigned ousrc0 = (((w >> 1) * 32) * 4096 + (w & 1) * 512 + lane * 8) * 2;

  const __amdgpu_buffer_rsrc_t orsrc = __builtin_amdgcn_make_buffer_rsrc((void*)ws, 0, 16 << 20, 0x00020000);

  const lptr kpair = lds0 + KBLK + w4 * 8192, vpair = lds0 + VBLK + w4 * 8192;
  u32x4 kvb[16]; unsigned wowarm = 0;
  const unsigned wowsrc = ((w * 16 + (lane >> 2)) * 512 + (lane & 3) * 32) * 2;
  auto issue_g = [&](auto GRP, auto KEY) {
    sfor<SG[GRP].n>([&](auto I) {
      constexpr Op o = SG[GRP].op[I];
      if constexpr (o.key == KEY && o.tag != TST && o.tag != TST + 1) {
        if constexpr (o.tag >= TOU) {
          constexpr int v = o.tag - TOU;
          if constexpr (v < 4) glds16<2>(ousrc0, ogb + (size_t)(2 * hp) * 4096 + (v & 3) * 1024, lds0 + OSL[v] * 16384 + w * 1024);
          else glds16<2>(ousrc, ogb + (size_t)hl[v >> 2] * 4096 + ((v & 3) * 2 + o.piece) * 512, lds0 + OSL[v] * 16384 + kh * 8192 + (2 * w4 + o.piece) * 1024);
        } else if constexpr (o.tag >= TWU) {
          constexpr int v = o.tag - TWU;
          glds16<POL_WO>(wusrc[o.piece], wub + (size_t)(hl[v >> 2] * 128 + (v & 3) * 32) * 512, lds0 + WSL[v] * 16384 + kh * 8192 + (2 * w4 + o.piece) * 1024);
        } else if constexpr (o.tag == TWARM + 1) {
        } else if constexpr (o.tag == TV + 1) {
          kvb[8 + o.piece] = __builtin_nontemporal_load((const u32x4*)((const char*)(vb0 + 4096 + (o.piece >> 2) * 64 + (o.piece & 3) * 1024) + vsrc));
        } else if constexpr (o.tag == TK + 1) {
          kvb[o.piece] = __builtin_nontemporal_load((const u32x4*)((const char*)(kb0 + 4096 + (o.piece >> 2) * 64 + (o.piece & 3) * 1024) + (ksrc ^ ((o.piece & 1) << 6))));
        } else if constexpr (o.tag >= TWARM) {
          if constexpr (o.piece < 8) glds16<POL_WQ>(warmq, wb0 + (size_t)(64 * rg + o.piece * 2) * 4096, vpair + (o.piece & 7) * 1024);
          else glds16<POL_X>(warm4, xb0 + 128 * hp + (o.piece - 8) * 4 * 512, vpair + (o.piece & 7) * 1024);
        } else if constexpr (o.tag >= TV) {
          glds16<1>(vsrc, vb0 + (o.tag - TV) * 4096 + (o.piece >> 2) * 64 + (o.piece & 3) * 1024, vpair + o.piece * 1024);
        } else if constexpr (o.tag >= TK) {
          glds16<1>(ksrc ^ ((o.piece & 1) << 6), kb0 + (o.tag - TK) * 4096 + (o.piece >> 2) * 64 + (o.piece & 3) * 1024, kpair + o.piece * 1024);
        } else {
          constexpr int u = o.tag - TU;
          if constexpr (o.piece < 4) glds16<POL_X>(srcA[o.piece], xb0 + 64 * (u & 7), lds0 + (u % 3) * 32768 + (4 * w4 + o.piece) * 1024);
          else glds16<POL_WQ>(srcB[o.piece - 4], wb0 + (u >> 3) * 128 + (size_t)64 * (u & 7) * 4096, lds0 + (u % 3) * 32768 + 16384 + (4 * w4 + o.piece - 4) * 1024);
        }
      }
    });
  };
  auto issue = [&](auto KEY) {
    if constexpr (KEY >= 240) issue_g(IC<0>{}, KEY);
    else { if (kh == 0) issue_g(IC<0>{}, KEY); else issue_g(IC<1>{}, KEY); }
  };
  issue(IC<0>{}); BARRIER(); issue(IC<1>{}); BARRIER(); issue(IC<2>{});
  ST(41);

  const int xrow = bi * 32 + r32;
  const unsigned xoff = xrow * 128 + ((h ^ ((xrow >> 1) & 7)) << 4);
  const unsigned woff = 16384 + (8 * h + q4) * 256 + ((2 * dh) ^ q4) * 64 + g16 * 32 + u4 * 8;
  const unsigned koff = r32 * 128 + ((h ^ ((r32 >> 1) & 7)) << 4);
  const unsigned voff = (4 * h + q4) * 128 + ((q4 >> 1) & 1) * 64 + g16 * 32 + u4 * 8;
  f32x16 acc[2];
  sfor<16>([&](auto U) {
    constexpr int u = U, k0 = 100 + u * 10, P = u >> 3;
    SB(); if (kh == 0) wait_v<w1wait(0, u)>(); else wait_v<w1wait(1, u)>(); BARRIER(); SB();
    ST(1 + u);
    if constexpr (u == 10) { if (tid == 0) __hip_atomic_store(sy1 + (1 + hp) * 16, (ep1 + 1), RLX_AGENT); }
    if constexpr ((u & 7) == 0) { acc[0] = f32x16{}; acc[1] = f32x16{}; }
    const lptr base = lds0 + (u % 3) * 32768;
    bf16x8 xb[3], wa[3][2];
    auto rd = [&](int ks) {
      xb[ks % 3] = ldsfrag(base + (xoff ^ (ks << 5)));
      wa[ks % 3][0] = trfrag(base + woff, ks * 4096, ks * 4096 + 1024);
      wa[ks % 3][1] = trfrag(base + (woff ^ 64), ks * 4096, ks * 4096 + 1024);
    };
    rd(0); rd(1);
    sfor<4>([&](auto KS) {
      constexpr int ks = KS;
      if constexpr (ks < 2) rd(ks + 2);
      issue(IC<k0 + ks>{});
      if constexpr (u == WOW_U && ks == 1) {
        if constexpr (POL_WO) asm volatile("global_load_dword %0, %1, %2 nt" : "=v"(wowarm) : "v"(wowsrc), "s"(wub + (size_t)(rg * 128) * 512) : "memory");
        else asm volatile("global_load_dword %0, %1, %2" : "=v"(wowarm) : "v"(wowsrc), "s"(wub + (size_t)(rg * 128) * 512) : "memory");
      }
      acc[0] = mfma(wa[ks % 3][0], xb[ks % 3], acc[0]);
      acc[1] = mfma(wa[ks % 3][1], xb[ks % 3], acc[1]);
    });
    if constexpr (u == 8 || u == 9) {
      if (kh) {
#pragma unroll
        for (int i = 0; i < 8; ++i) *(LDS_AS u32x4*)((u == 8 ? kpair : vpair) + i * 1024 + lane * 16) = kvb[(u - 8) * 8 + i];
        asm volatile("s_waitcnt lgkmcnt(0)" ::: "memory");
      }
    }
    if constexpr (u == 15) asm volatile("" :: "v"(wowarm));
    SB();
    if constexpr ((u & 7) == 7) {
      ST(17 + 3 * P);
      f32x16 sc = f32x16{};
#pragma unroll
      for (int t = 0; t < 2; ++t)
#pragma unroll
        for (int s2 = 0; s2 < 2; ++s2) sc = mfma(ldsfrag(kreg + (koff ^ ((2 * t + s2) << 5))), as_bf(pack8(acc[t], s2)), sc);
#pragma unroll
      for (int q = 0; q < 4; ++q) {
        f32x4 v; v[0] = sc[4 * q]; v[1] = sc[4 * q + 1]; v[2] = sc[4 * q + 2]; v[3] = sc[4 * q + 3];
        *(LDS_AS f32x4*)(kreg + q * 1024 + lane * 16) = v;
      }
      asm volatile("s_waitcnt lgkmcnt(0)" : "+s"(ep1), "+s"(ep3) :: "memory"); BARRIER(); SB();
      issue(IC<k0 + 8>{});
      const lptr oth = lds0 + KBLK + (w ^ 1) * 4096 + lane * 16;
#pragma unroll
      for (int q = 0; q < 4; ++q) {
        const f32x4 v = *(LDS_AS f32x4*)(oth + q * 1024);
#pragma unroll
        for (int e = 0; e < 4; ++e) sc[4 * q + e] += v[e];
      }
      constexpr float SCALE = 0.08838834764831845f, LOG2E = 1.4426950408889634f;
      float m = -3.0e38f;
#pragma unroll
      for (int i = 0; i < 16; ++i) { sc[i] = bfr(bfr(sc[i]) * SCALE); m = fmaxf(m, sc[i]); }
      m = fmaxf(m, __shfl_xor(m, 32));
      float sum = 0.f;
#pragma unroll
      for (int i = 0; i < 16; ++i) { sc[i] = __builtin_amdgcn_exp2f((sc[i] - m) * LOG2E); sum += sc[i]; }
      sum += __shfl_xor(sum, 32);
      const float inv = 1.0f / sum;
#pragma unroll
      for (int i = 0; i < 16; ++i) sc[i] *= inv;
      const bf16x8 pf0 = as_bf(pack8(sc, 0)), pf1 = as_bf(pack8(sc, 1));
      ST(18 + 3 * P);
      const unsigned ooff = (((br * 32 + b) * 32 + head0 + P) * 8 + 4 * dh) * 1024 + lane * 16;
      f32x16 o[2];
#pragma unroll
      for (int t = 0; t < 2; ++t) {
        o[t] = f32x16{};
        o[t] = mfma(trfrag(vreg + (voff ^ (t << 6)), 0, 1024), pf0, o[t]);
        o[t] = mfma(trfrag(vreg + (voff ^ (t << 6)), 2048, 2048 + 1024), pf1, o[t]);
      }
#pragma unroll
      for (int t = 0; t < 2; ++t) {
        const u32x4 o0 = pack8(o[t], 0), o1 = pack8(o[t], 1);
        __builtin_amdgcn_raw_buffer_store_b128(o0, orsrc, ooff + (2 * t) * 1024, 0, 16);
        __builtin_amdgcn_raw_buffer_store_b128(o1, orsrc, ooff + (2 * t + 1) * 1024, 0, 16);
        if constexpr (P == 1) {
          *(LDS_AS u32x4*)(vreg + (2 * t) * 1024 + lane * 16) = o0;
          *(LDS_AS u32x4*)(vreg + (2 * t + 1) * 1024 + lane * 16) = o1;
        }
      }
      ST(19 + 3 * P);
    }
  });
  f32x16 ac2[2][2];
  {
#pragma unroll
    for (int a = 0; a < 2; ++a)
#pragma unroll
      for (int c = 0; c < 2; ++c) ac2[a][c] = f32x16{};
    const unsigned woff2 = kh * 8192 + (8 * h + q4) * 256 + ((2 * wn) ^ q4) * 64 + g16 * 32 + u4 * 8;
    const unsigned ooff = kh * 8192 + (wr * 2) * 2048 + lane * 16;
    sfor<16>([&](auto V) {
      constexpr int v = V, k0 = 300 + v * 10;
      if constexpr (v == 0) { if (w == 0) poll3(sy1 + 16, (ep1 + 1), lane, hp); }
      if constexpr (v == 5) {
        if (w == 0) { poll3(sy1 + 80, (ep1 + 1), lane, hp); if (hp == 0 && lane == 0) __hip_atomic_store(sy1, (ep1 + 1), RLX_AGENT); }
      }
      SB();
      if constexpr (v == 0) asm volatile("s_waitcnt lgkmcnt(0)" ::: "memory");
      wait_v<swait(0, k0, TWU + v, TOU + v, v == 2 ? TST + 1 : -1)>(); BARRIER(); SB();
      ST(23 + v);
      if constexpr (v == 2) { if (tid == 0) __hip_atomic_store(sy1 + (5 + hp) * 16, (ep1 + 1), RLX_AGENT); }
      sfor<2>([&](auto KS) {
        constexpr int ks = KS;
        bf16x8 wa[2], ob[2];
        const lptr wbase = lds0 + WSL[v] * 16384;
#pragma unroll
        for (int t = 0; t < 2; ++t) wa[t] = trfrag(wbase + (woff2 ^ (t << 6)), ks * 4096, ks * 4096 + 1024);
        if constexpr (v < 4) {
          constexpr int f = 2 * v + ks;
          const lptr p0 = kh ? lds0 + VBLK + (wr * 4 + (f >> 2)) * 4096 + (f & 3) * 1024 + lane * 16 : lds0 + OSL[v] * 16384 + wr * 4096 + ks * 1024 + lane * 16;
          ob[0] = ldsfrag(p0); ob[1] = ldsfrag(p0 + (kh ? 8192 : 2048));
        } else {
#pragma unroll
          for (int t = 0; t < 2; ++t) ob[t] = ldsfrag(lds0 + OSL[v] * 16384 + ooff + t * 2048 + ks * 1024);
        }
        issue(IC<k0 + 2 * ks>{});
        ac2[0][0] = mfma(wa[0], ob[0], ac2[0][0]);
        ac2[0][1] = mfma(wa[0], ob[1], ac2[0][1]);
        issue(IC<k0 + 2 * ks + 1>{});
        ac2[1][0] = mfma(wa[1], ob[0], ac2[1][0]);
        ac2[1][1] = mfma(wa[1], ob[1], ac2[1][1]);
      });
      SB();
    });
  }
  ST(39);
  f32x16 fin[2];
  {
    const lptr my = lds0 + w * 8192 + lane * 16, other = lds0 + (w ^ 4) * 8192 + lane * 16;
#pragma unroll
    for (int c = 0; c < 2; ++c)
#pragma unroll
      for (int q = 0; q < 4; ++q) {
        f32x4 v;
#pragma unroll
        for (int e = 0; e < 4; ++e) v[e] = kh ? ac2[0][c][4 * q + e] : ac2[1][c][4 * q + e];
        *(LDS_AS f32x4*)(my + (c * 4 + q) * 1024) = v;
      }
    asm volatile("s_waitcnt lgkmcnt(0)" ::: "memory"); BARRIER(); SB();
#pragma unroll
    for (int c = 0; c < 2; ++c)
#pragma unroll
      for (int q = 0; q < 4; ++q) {
        f32x4 v = *(LDS_AS f32x4*)(other + (c * 4 + q) * 1024);
#pragma unroll
        for (int e = 0; e < 4; ++e) fin[c][4 * q + e] = v[e] + (kh ? ac2[1][c][4 * q + e] : ac2[0][c][4 * q + e]);
      }
  }
  ST(40);
  {
    const __amdgpu_buffer_rsrc_t srsrc = __builtin_amdgcn_make_buffer_rsrc((void*)(ws + WS_SLAB), 0, 16 << 20, 0x00020000);
    const unsigned gbase = (unsigned)((br * 8 + rg) * 4 + hp) * 262144u + (wn * 2 + kh) * 4096 + lane * 16;
#pragma unroll
    for (int c = 0; c < 2; ++c) {
      const int bq = 2 * wr + c;
      if (bq != ho) {
#pragma unroll
        for (int q = 0; q < 4; ++q) {
          f32x4 v;
#pragma unroll
          for (int e = 0; e < 4; ++e) v[e] = fin[c][4 * q + e];
          __builtin_amdgcn_raw_buffer_store_b128(__builtin_bit_cast(u32x4, v), srsrc, gbase + bq * 65536 + ho * 16384 + q * 1024, 0, 16);
        }
      }
    }
    asm volatile("s_waitcnt vmcnt(0)" ::: "memory"); BARRIER(); SB();
    if (tid == 0) __hip_atomic_store(sy3 + (1 + ho) * 16, (ep3 + 1), RLX_AGENT);
    ST(42);
    if (wr == (ho >> 1)) {
      poll3(sy3 + 16, (ep3 + 1), lane, ho);
      if (ho == 0 && tid == 0) __hip_atomic_store(sy3, (ep3 + 1), RLX_AGENT);
      ST(43);
      f32x16 s = (ho & 1) ? fin[1] : fin[0];
      u32x4 ld[3][4];
#pragma unroll
      for (int i = 0; i < 3; ++i) {
        const int src = (ho + 1 + i) & 3;
#pragma unroll
        for (int q = 0; q < 4; ++q) ld[i][q] = __builtin_amdgcn_raw_buffer_load_b128(srsrc, gbase + ho * 65536 + src * 16384 + q * 1024, 0, 16);
      }
#pragma unroll
      for (int i = 0; i < 3; ++i)
#pragma unroll
        for (int q = 0; q < 4; ++q) {
          const f32x4 v = __builtin_bit_cast(f32x4, ld[i][q]);
#pragma unroll
          for (int e = 0; e < 4; ++e) s[4 * q + e] += v[e];
        }
      u16* dst = out + (size_t)(rg * 128 + ho * 32 + r32) * 1024 + br * 512 + hp * 128 + wn * 64 + kh * 32 + 8 * h;
      *(u32x4*)(dst) = pack8(s, 0);
      *(u32x4*)(dst + 16) = pack8(s, 1);
    }
  }
  ST(44);
  ST_OUT(ws, 46);
}

extern "C" void kernel_launch(void* const* d_in, const int* in_sizes, int n_in, void* d_out, int out_size, void* d_ws, size_t ws_size, hipStream_t stream) {
  (void)hipFuncSetAttribute((const void*)fused, hipFuncAttributeMaxDynamicSharedMemorySize, LDS_BYTES);
  fused<<<256, 512, LDS_BYTES, stream>>>((const u16*)d_in[0], (const u16*)d_in[1], (const u16*)d_in[2], (const u16*)d_in[3], (const u16*)d_in[4],
                                         (const u16*)d_in[5], (const u16*)d_in[6], (const u16*)d_in[7], (const u16*)d_in[8], (const u16*)d_in[9],
                                         (char*)d_ws, (u16*)d_out);
}
```

```cpp
#include <hip/hip_runtime.h>
#include <hip/hip_bf16.h>
#include <utility>

typedef unsigned short u16;
typedef unsigned long long u64;
typedef __attribute__((ext_vector_type(4))) short s16x4;
typedef __attribute__((ext_vector_type(8))) short s16x8;
typedef __attribute__((ext_vector_type(8))) __bf16 bf16x8;
typedef __attribute__((ext_vector_type(2))) __bf16 bf16x2;
typedef __attribute__((ext_vector_type(2))) float f32x2;
typedef __attribute__((ext_vector_type(4))) float f32x4;
typedef __attribute__((ext_vector_type(16))) float f32x16;
typedef __attribute__((ext_vector_type(4))) unsigned u32x4;
#define LDS_AS __attribute__((address_space(3)))
#define GAS __attribute__((address_space(1)))
#define DI __device__ __forceinline__
typedef LDS_AS char* lptr;
typedef GAS u64 gu64;
#define RLX_AGENT __ATOMIC_RELAXED, __HIP_MEMORY_SCOPE_AGENT

#define CODE_PF 25088
#define POL_ALL 0
#define WARM_NT 1
#define USE_ACQ 0
#define POLL_V 2

template <int N> DI void wait_v() { asm volatile("s_waitcnt vmcnt(%0)" ::"n"(N) : "memory"); }
#define SB() __builtin_amdgcn_sched_barrier(0)

DI bf16x8 as_bf(s16x8 v) { return __builtin_bit_cast(bf16x8, v); }
DI bf16x8 as_bf(u32x4 v) { return __builtin_bit_cast(bf16x8, v); }
DI unsigned pk2(float a, float b) { bf16x2 r = __builtin_convertvector(f32x2{a, b}, bf16x2); return __builtin_bit_cast(unsigned, r); }
DI float bfr(float a) { bf16x2 r = __builtin_convertvector(f32x2{a, a}, bf16x2); return __uint_as_float(__builtin_bit_cast(unsigned, r) << 16); }
DI f32x16 mfma(bf16x8 a, bf16x8 b, f32x16 c) { return __builtin_amdgcn_mfma_f32_32x32x16_bf16(a, b, c, 0, 0, 0); }
DI s16x4 trread(lptr addr) { return __builtin_amdgcn_ds_read_tr16_b64_v4i16((LDS_AS s16x4*)addr); }
DI bf16x8 trfrag(lptr addr, int off0, int off1) {
  s16x4 lo = trread(addr + off0), hi = trread(addr + off1);
  return as_bf(__builtin_shufflevector(lo, hi, 0, 1, 2, 3, 4, 5, 6, 7));
}
DI bf16x8 ldsfrag(lptr addr) { return *(LDS_AS bf16x8*)addr; }
template <int POL> DI void glds16(unsigned voff, const void* sbase, lptr lds_addr) {
  unsigned keep; const unsigned dst = (unsigned)(size_t)lds_addr;
  if constexpr (POL == 1)
    asm volatile("s_mov_b32 %0, m0\n\ts_mov_b32 m0, %2\n\ts_nop 0\n\tglobal_load_lds_dwordx4 %1, %3 nt\n\ts_mov_b32 m0, %0"
                 : "=&s"(keep) : "v"(voff), "s"(dst), "s"(sbase) : "memory");
  else if constexpr (POL == 2)
    asm volatile("s_mov_b32 %0, m0\n\ts_mov_b32 m0, %2\n\ts_nop 0\n\tglobal_load_lds_dwordx4 %1, %3 sc1\n\ts_mov_b32 m0, %0"
                 : "=&s"(keep) : "v"(voff), "s"(dst), "s"(sbase) : "memory");
  else
    asm volatile("s_mov_b32 %0, m0\n\ts_mov_b32 m0, %2\n\ts_nop 0\n\tglobal_load_lds_dwordx4 %1, %3\n\ts_mov_b32 m0, %0"
                 : "=&s"(keep) : "v"(voff), "s"(dst), "s"(sbase) : "memory");
}
DI u32x4 ldpol(const u32x4* p) { if constexpr (POL_ALL) return __builtin_nontemporal_load(p); else return *p; }
DI u32x4 pack8(const f32x16& a, int s) {
  u32x4 r;
  r[0] = pk2(a[8 * s + 0], a[8 * s + 1]); r[1] = pk2(a[8 * s + 2], a[8 * s + 3]);
  r[2] = pk2(a[8 * s + 4], a[8 * s + 5]); r[3] = pk2(a[8 * s + 6], a[8 * s + 7]);
  return r;
}
template <int... Is, class F> DI void sfor_impl(std::integer_sequence<int, Is...>, F&& f) { (f(std::integral_constant<int, Is>{}), ...); }
template <int N, class F> DI void sfor(F&& f) { sfor_impl(std::make_integer_sequence<int, N>{}, f); }

#define ST_DECL(n)
#define ST(i)
#define ST_OUT(ws, n)

struct Op { int it, pos, tag, piece; };
enum { TX = 100, TW = 200, TKV = 300, TWARM = 400, TWU = 500, TOU = 600 };
struct Sched { Op op[128]; int n; };
constexpr Sched make1() {
  Sched s{}; int n = 0;
  const int pro[9] = {TX + 0, TW + 0, TW + 1, TX + 1, TW + 2, TW + 3, TX + 2, TW + 4, TW + 5};
  for (int i = 0; i < 9; ++i) { s.op[n++] = Op{-1, 0, pro[i], 0}; s.op[n++] = Op{-1, 0, pro[i], 1}; }
  for (int u = 0; u < 16; ++u) {
    const bool hw = u >= 1 && u <= 10, hx = u >= 2 && u <= 10 && !(u & 1), hkv = u <= 7, hwm = u >= 2 && u <= 5;
    if (hw) s.op[n++] = Op{u, 0, TW + u + 5, 0};
    if (hx) s.op[n++] = Op{u, 1, TX + u / 2 + 2, 0};
    if (hkv) s.op[n++] = Op{u, 1, TKV, 2 * u};
    if (hw) s.op[n++] = Op{u, 2, TW + u + 5, 1};
    if (hx) s.op[n++] = Op{u, 3, TX + u / 2 + 2, 1};
    if (hkv) s.op[n++] = Op{u, 3, TKV, 2 * u + 1};
    if (hwm) s.op[n++] = Op{u, 3, TWARM, u - 2};
  }
  s.n = n; return s;
}
constexpr Sched S1 = make1();
constexpr int swait(const Sched& s, int it, int tagA, int tagB) {
  int issued = 0, last = -1;
  for (int i = 0; i < s.n; ++i) if (s.op[i].it < it) { if (s.op[i].tag == tagA || s.op[i].tag == tagB) last = i; issued = i + 1; }
  if (last < 0) return 63;
  const int r = issued - 1 - last; return r > 63 ? 63 : r;
}
constexpr int WSL[16] = {3, 7, 0, 1, 6, 3, 0, 9, 5, 6, 3, 0, 9, 5, 6, 3};
constexpr int OSL[16] = {-1, -1, -1, -1, 2, 7, 1, 4, 8, 2, 7, 1, 4, 8, 2, 7};
constexpr Sched make2() {
  Sched s{}; int n = 0;
  auto add = [&](int v, int pos, int tag) { s.op[n++] = Op{v, pos, tag, 0}; s.op[n++] = Op{v, pos + 1 > 3 ? 3 : pos + 1, tag, 1}; };
  add(0, 0, TWU + 2); add(0, 2, TWU + 3); add(0, 3, TWU + 4);
  add(1, 0, TWU + 5);
  add(2, 0, TOU + 4); add(2, 2, TOU + 5);
  add(3, 0, TWU + 6);
  add(4, 0, TOU + 6); add(4, 1, TWU + 7); add(4, 2, TOU + 7); add(4, 3, TWU + 8); add(4, 3, TOU + 8);
  for (int v = 5; v <= 11; ++v) { add(v, 0, TWU + v + 4); add(v, 2, TOU + v + 4); }
  for (int i = 1; i < n; ++i) { Op o = s.op[i]; int j = i - 1; while (j >= 0 && (s.op[j].it > o.it || (s.op[j].it == o.it && s.op[j].pos > o.pos))) { s.op[j + 1] = s.op[j]; --j; } s.op[j + 1] = o; }
  s.n = n; return s;
}
constexpr Sched S2 = make2();

constexpr int LDS_BYTES = 163840;
constexpr size_t WS_SLAB = 16u << 20, WS_SYNC = 32u << 20;

DI bool poll3(gu64* f0, u64 tag, int lane, int skip) {
  for (unsigned spins = 0; spins < (1u << 22); ++spins) {
    u64 v = tag;
    if (lane < 4 && lane != skip) v = __hip_atomic_load(f0 + lane * 16, RLX_AGENT);
    if (__all(v == tag)) return true;
    __builtin_amdgcn_s_sleep(1);
  }
  return false;
}

__global__ __launch_bounds__(512, 2) void fused(const u16* __restrict__ xA, const u16* __restrict__ kA, const u16* __restrict__ vA, const u16* __restrict__ wqA, const u16* __restrict__ woA,
                                                const u16* __restrict__ xB, const u16* __restrict__ kB, const u16* __restrict__ vB, const u16* __restrict__ wqB, const u16* __restrict__ woB,
                                                char* __restrict__ ws, u16* __restrict__ out) {
  extern __shared__ __attribute__((aligned(16))) char smem[];
  const lptr lds0 = (lptr)smem;
  const int id = blockIdx.x, g = id & 7, j = id >> 3;
  const int br = g >> 2, ho = g & 3, rg = j >> 2, hp = j & 3;
  const int tid = threadIdx.x, lane = tid & 63;
  const int w = __builtin_amdgcn_readfirstlane(tid >> 6);
  const u16* x = br ? xB : xA; const u16* kc = br ? kB : kA; const u16* vc = br ? vB : vA; const u16* wq = br ? wqB : wqA; const u16* wo = br ? woB : woA;
  ST_DECL(44); ST(0);
  unsigned cpf = 0;
  {
    const char* pc = (const char*)__builtin_amdgcn_s_getpc();
    const unsigned line = (unsigned)(j * 8 + w) * 64 + lane;
    if (w == 7 - (j & 7) && (unsigned)((j & 3) * 64 + lane) * 128 < CODE_PF)
      asm volatile("global_load_dword %0, %1, %2" : "=v"(cpf) : "v"((unsigned)((j & 3) * 64 + lane) * 128), "s"(pc) : "memory");
    (void)line;
  }
  gu64* sy1 = (gu64*)(ws + WS_SYNC) + (size_t)((br * 4 + ho) * 8 + rg) * 128;
  gu64* sy3 = (gu64*)(ws + WS_SYNC) + (size_t)(64 + (br * 8 + rg) * 4 + hp) * 128;
  const u64 tag1 = __hip_atomic_load(sy1, RLX_AGENT) + 1, tag3 = __hip_atomic_load(sy3, RLX_AGENT) + 1;

  const int r32 = lane & 31, h = lane >> 5;
  const int q4 = (lane & 15) >> 2, p4 = lane & 3, g16 = (lane >> 4) & 1, u4 = ((p4 & 1) << 1) | (p4 >> 1);
  const int head0 = ho * 8 + hp * 2;
  f32x16 acc[4];
  {
    const int bi = w >> 1, hi = w & 1;
    const int b = rg * 4 + bi, head = head0 + hi;
    unsigned xsrc[2], wsrc[2];
#pragma unroll
    for (int i = 0; i < 2; ++i) {
      const int row = (2 * w + i) * 8 + (lane >> 3), c = (lane & 7) ^ ((row >> 1) & 7);
      xsrc[i] = (row * 512 + 8 * c) * 2;
      const int kr = (2 * w + i) * 2 + (lane >> 5), c2 = (lane & 31) ^ ((kr & 3) << 2);
      wsrc[i] = (kr * 4096 + 8 * c2) * 2;
    }
    const u16* xb0 = x + (size_t)rg * 128 * 512;
    const u16* wb0 = wq + head0 * 128;
    const unsigned warmsrc = (((w * 16 + (lane >> 4)) * 512) + (lane & 15) * 8) * 2;
    const u16* warmb = wo + (size_t)(ho * 1024 + rg * 128) * 512 + hp * 128;
    const u16* kbase = kc + (size_t)(b * 32 + head) * 4096 + lane * 8;
    const u16* vbase = vc + (size_t)(b * 32 + head) * 4096 + lane * 8;
    u32x4 kv[16];
    auto issue = [&](auto IT, auto POS) {
      sfor<S1.n>([&](auto I) {
        constexpr Op o = S1.op[I];
        if constexpr (o.it == IT && o.pos == POS) {
          if constexpr (o.tag >= TWARM) glds16<POL_ALL>(warmsrc, warmb + o.piece * 4 * 512, lds0 + 9 * 16384 + w * 2048 + (o.piece & 1) * 1024);
          else if constexpr (o.tag >= TKV) {
            if constexpr (o.piece < 8) kv[o.piece] = ldpol((const u32x4*)(kbase + o.piece * 512));
            else kv[o.piece] = ldpol((const u32x4*)(vbase + (o.piece - 8) * 512));
          } else if constexpr (o.tag >= TW) glds16<POL_ALL>(wsrc[o.piece], wb0 + (size_t)32 * (o.tag - TW) * 4096, lds0 + ((o.tag - TW) % 6) * 16384 + (2 * w + o.piece) * 1024);
          else glds16<POL_ALL>(xsrc[o.piece], xb0 + 64 * (o.tag - TX), lds0 + (6 + (o.tag - TX) % 3) * 16384 + (2 * w + o.piece) * 1024);
        }
      });
    };
    issue(std::integral_constant<int, -1>{}, std::integral_constant<int, 0>{});

    const int xrow = bi * 32 + r32;
    const unsigned xoff = 6 * 16384 + xrow * 128 + ((h ^ ((xrow >> 1) & 7)) << 4);
    const unsigned woff = (8 * h + q4) * 512 + (4 * hi + q4) * 64 + g16 * 32 + u4 * 8;
    const int kslot = (bi == 0) ? 9 : (bi == 1) ? 4 : (bi == 2) ? 5 : 8, vslot = (bi == 0) ? 0 : (bi == 1) ? 1 : (bi == 2) ? 6 : 2;
    const lptr karea = lds0 + kslot * 16384 + hi * 8192, varea = lds0 + vslot * 16384 + hi * 8192;
#pragma unroll
    for (int t = 0; t < 4; ++t) acc[t] = f32x16{};
    bf16x8 xb[2], wa[2][4];
    auto rdfrags = [&](int kk) {
      const int u = kk >> 1, ks = kk & 1, X = kk >> 2, ks4 = kk & 3, buf = kk & 1;
      xb[buf] = ldsfrag(lds0 + (X % 3) * 16384 + (xoff ^ (ks4 << 5)));
#pragma unroll
      for (int t = 0; t < 4; ++t) wa[buf][t] = trfrag(lds0 + (u % 6) * 16384 + (woff ^ (t << 6)), ks * 8192, ks * 8192 + 2048);
    };
    auto parkK = [&]() {
#pragma unroll
      for (int i = 0; i < 8; ++i) { const int row = 4 * i + (lane >> 4); *(LDS_AS u32x4*)(karea + row * 256 + (((lane & 15) ^ (row & 15)) << 4)) = kv[i]; }
    };
    auto parkV = [&]() {
#pragma unroll
      for (int i = 0; i < 8; ++i) { const int row = 4 * i + (lane >> 4); *(LDS_AS u32x4*)(varea + row * 256 + (((lane & 15) ^ ((row & 3) << 2)) << 4)) = kv[8 + i]; }
    };
    sfor<16>([&](auto U) {
      constexpr int u = U;
      SB(); wait_v<swait(S1, u, TW + u + 1, TX + (u + 1) / 2)>(); asm volatile("s_barrier" ::: "memory"); SB();
      ST(1 + u);
      if constexpr (u == 0) rdfrags(0);
      rdfrags(2 * u + 1);
      issue(U, std::integral_constant<int, 0>{});
      acc[0] = mfma(wa[0][0], xb[0], acc[0]); acc[1] = mfma(wa[0][1], xb[0], acc[1]);
      issue(U, std::integral_constant<int, 1>{});
      acc[2] = mfma(wa[0][2], xb[0], acc[2]); acc[3] = mfma(wa[0][3], xb[0], acc[3]);
      SB();
      if constexpr (u < 15) rdfrags(2 * u + 2);
      issue(U, std::integral_constant<int, 2>{});
      acc[0] = mfma(wa[1][0], xb[1], acc[0]); acc[1] = mfma(wa[1][1], xb[1], acc[1]);
      issue(U, std::integral_constant<int, 3>{});
      acc[2] = mfma(wa[1][2], xb[1], acc[2]); acc[3] = mfma(wa[1][3], xb[1], acc[3]);
      SB();
      if constexpr (u == 10) { if (bi == 0) parkK(); }
      if constexpr (u == 11) { if (bi == 1) parkK(); }
      if constexpr (u == 12) { if (bi >= 2) parkK(); }
      if constexpr (u == 13) { if (bi == 0) parkV(); }
      if constexpr (u == 14) { if (bi == 1 || bi == 2) parkV(); }
      if constexpr (u == 15) { if (bi == 3) parkV(); }
    });
    asm volatile("" :: "v"(cpf));
    SB(); asm volatile("s_barrier" ::: "memory"); SB();
    ST(17);
  }
  const int kh = w >> 2, wn = (w >> 1) & 1, wr = w & 1, w4 = w & 3;
  unsigned wusrc[2];
#pragma unroll
  for (int i = 0; i < 2; ++i) {
    const int kr = (2 * w4 + i) * 4 + (lane >> 4), c = (lane & 15) ^ ((kr & 3) << 2);
    wusrc[i] = ((kh * 128 + kr) * 512 + 8 * c) * 2;
  }
  const u16* wub = wo + (size_t)(ho * 1024) * 512 + hp * 128;
  const unsigned ousrc = (((w4 * 32 + kh) * 8 * 64 + lane) * 8) * 2;
  const u16* oub = (const u16*)ws + (size_t)((br * 32 + rg * 4) * 32 + ho * 8) * 4096;
  auto ldWU = [&](int v, int i) {
    const int hq = (hp + (v >> 2)) & 3;
    glds16<POL_ALL>(wusrc[i], wub + (size_t)(hq * 256 + (v & 3) * 32) * 512, lds0 + WSL[v] * 16384 + kh * 8192 + (2 * w4 + i) * 1024);
  };
  auto ldOU = [&](int v, int i) {
    const int hq = (hp + (v >> 2)) & 3;
    glds16<2>(ousrc, oub + (size_t)(hq * 2) * 4096 + ((v & 3) * 2 + i) * 512, lds0 + OSL[v] * 16384 + kh * 8192 + (2 * w4 + i) * 1024);
  };
  ldWU(0, 0); ldWU(0, 1); ldWU(1, 0); ldWU(1, 1);
  {
    const int bi = w >> 1, hi = w & 1;
    const int b = rg * 4 + bi, head = head0 + hi;
    const int kslot = (bi == 0) ? 9 : (bi == 1) ? 4 : (bi == 2) ? 5 : 8, vslot = (bi == 0) ? 0 : (bi == 1) ? 1 : (bi == 2) ? 6 : 2;
    const lptr karea = lds0 + kslot * 16384 + hi * 8192, varea = lds0 + vslot * 16384 + hi * 8192;
    const unsigned koff = r32 * 256 + ((h ^ (r32 & 15)) << 4);
    f32x16 sc = f32x16{};
#pragma unroll
    for (int t = 0; t < 4; ++t)
#pragma unroll
      for (int s2 = 0; s2 < 2; ++s2) sc = mfma(ldsfrag(karea + (koff ^ ((2 * t + s2) << 5))), as_bf(pack8(acc[t], s2)), sc);
    constexpr float SCALE = 0.08838834764831845f, LOG2E = 1.4426950408889634f;
    float m = -3.0e38f;
#pragma unroll
    for (int i = 0; i < 16; ++i) { sc[i] = bfr(bfr(sc[i]) * SCALE); m = fmaxf(m, sc[i]); }
    m = fmaxf(m, __shfl_xor(m, 32));
    float sum = 0.f;
#pragma unroll
    for (int i = 0; i < 16; ++i) { sc[i] = __builtin_amdgcn_exp2f((sc[i] - m) * LOG2E); sum += sc[i]; }
    sum += __shfl_xor(sum, 32);
    const float inv = 1.0f / sum;
#pragma unroll
    for (int i = 0; i < 16; ++i) sc[i] *= inv;
    const bf16x8 pf0 = as_bf(pack8(sc, 0)), pf1 = as_bf(pack8(sc, 1));
    const unsigned voff = (4 * h + q4) * 256 + q4 * 64 + g16 * 32 + u4 * 8;
    const __amdgpu_buffer_rsrc_t orsrc = __builtin_amdgcn_make_buffer_rsrc((void*)ws, 0, 16 << 20, 0x00020000);
    const unsigned ooff = (((br * 32 + b) * 32 + head) * 8 * 64 + lane) * 16;
#pragma unroll
    for (int t = 0; t < 4; ++t) {
      f32x16 o = f32x16{};
      o = mfma(trfrag(varea + (voff ^ (t << 6)), 0, 2048), pf0, o);
      o = mfma(trfrag(varea + (voff ^ (t << 6)), 4096, 4096 + 2048), pf1, o);
      const u32x4 o0 = pack8(o, 0), o1 = pack8(o, 1);
      __builtin_amdgcn_raw_buffer_store_b128(o0, orsrc, ooff + (2 * t) * 1024, 0, 16);
      __builtin_amdgcn_raw_buffer_store_b128(o1, orsrc, ooff + (2 * t + 1) * 1024, 0, 16);
      *(LDS_AS u32x4*)(karea + (2 * t) * 1024 + lane * 16) = o0;
      *(LDS_AS u32x4*)(karea + (2 * t + 1) * 1024 + lane * 16) = o1;
    }
  }
  ST(18);
  f32x16 ac2[2][2];
  {
#pragma unroll
    for (int a = 0; a < 2; ++a)
#pragma unroll
      for (int c = 0; c < 2; ++c) ac2[a][c] = f32x16{};
    const unsigned woff = kh * 8192 + (8 * h + q4) * 256 + ((2 * wn) ^ q4) * 64 + g16 * 32 + u4 * 8;
    const unsigned ooff = kh * 8192 + (wr * 2) * 2048 + lane * 16;
    const lptr own0 = lds0 + (wr ? 5 : 9) * 16384 + kh * 8192 + lane * 16, own1 = lds0 + (wr ? 8 : 4) * 16384 + kh * 8192 + lane * 16;
    auto issue2 = [&](auto V, auto POS) {
      sfor<S2.n>([&](auto I) {
        constexpr Op o = S2.op[I];
        if constexpr (o.it == V && o.pos == POS) {
          if constexpr (o.tag >= TOU) ldOU(o.tag - TOU, o.piece); else ldWU(o.tag - TWU, o.piece);
        }
      });
    };
    sfor<16>([&](auto V) {
      constexpr int v = V;
      if constexpr (v == 0) {
        SB(); asm volatile("s_waitcnt vmcnt(0) lgkmcnt(0)" ::: "memory"); asm volatile("s_barrier" ::: "memory"); SB();
        if (tid == 0) __hip_atomic_store(sy1 + (1 + hp) * 16, tag1, RLX_AGENT);
      } else {
        if constexpr (v == POLL_V) {
          if (w == 0) {
            poll3(sy1 + 16, tag1, lane, hp);
            if constexpr (USE_ACQ) { __builtin_amdgcn_fence(__ATOMIC_ACQUIRE, "agent"); asm volatile("s_waitcnt vmcnt(0)" ::: "memory"); }
            if (hp == 0 && lane == 0) __hip_atomic_store(sy1, tag1, RLX_AGENT);
          }
        }
        SB(); wait_v<swait(S2, v, TWU + v, TOU + v)>(); asm volatile("s_barrier" ::: "memory"); SB();
      }
      ST(19 + v);
      sfor<2>([&](auto KS) {
        constexpr int ks = KS;
        bf16x8 wa[2], ob[2];
        const lptr wbase = lds0 + WSL[v] * 16384;
#pragma unroll
        for (int t = 0; t < 2; ++t) wa[t] = trfrag(wbase + (woff ^ (t << 6)), ks * 4096, ks * 4096 + 1024);
        if constexpr (v < 4) { ob[0] = ldsfrag(own0 + (2 * v + ks) * 1024); ob[1] = ldsfrag(own1 + (2 * v + ks) * 1024); }
        else {
#pragma unroll
          for (int t = 0; t < 2; ++t) ob[t] = ldsfrag(lds0 + OSL[v] * 16384 + ooff + t * 2048 + ks * 1024);
        }
        issue2(V, std::integral_constant<int, 2 * ks>{});
        ac2[0][0] = mfma(wa[0], ob[0], ac2[0][0]);
        ac2[0][1] = mfma(wa[0], ob[1], ac2[0][1]);
        issue2(V, std::integral_constant<int, 2 * ks + 1>{});
        ac2[1][0] = mfma(wa[1], ob[0], ac2[1][0]);
        ac2[1][1] = mfma(wa[1], ob[1], ac2[1][1]);
      });
      SB();
    });
  }
  ST(35);
  f32x16 fin[2];
  {
    const int es = (w >> 1) + ((w >> 1) == 3), eo = ((w ^ 4) >> 1) + (((w ^ 4) >> 1) == 3);
    const lptr my = lds0 + es * 16384 + (w & 1) * 8192 + lane * 16, other = lds0 + eo * 16384 + (w & 1) * 8192 + lane * 16;
#pragma unroll
    for (int c = 0; c < 2; ++c)
#pragma unroll
      for (int q = 0; q < 4; ++q) {
        f32x4 v;
#pragma unroll
        for (int e = 0; e < 4; ++e) v[e] = kh ? ac2[0][c][4 * q + e] : ac2[1][c][4 * q + e];
        *(LDS_AS f32x4*)(my + (c * 4 + q) * 1024) = v;
      }
    asm volatile("s_waitcnt lgkmcnt(0)" ::: "memory"); asm volatile("s_barrier" ::: "memory"); SB();
#pragma unroll
    for (int c = 0; c < 2; ++c)
#pragma unroll
      for (int q = 0; q < 4; ++q) {
        f32x4 v = *(LDS_AS f32x4*)(other + (c * 4 + q) * 1024);
#pragma unroll
        for (int e = 0; e < 4; ++e) fin[c][4 * q + e] = v[e] + (kh ? ac2[1][c][4 * q + e] : ac2[0][c][4 * q + e]);
      }
  }
  ST(36);
  {
    const __amdgpu_buffer_rsrc_t srsrc = __builtin_amdgcn_make_buffer_rsrc((void*)(ws + WS_SLAB), 0, 16 << 20, 0x00020000);
    const unsigned gbase = (unsigned)((br * 8 + rg) * 4 + hp) * 262144u + (wn * 2 + kh) * 4096 + lane * 16;
#pragma unroll
    for (int c = 0; c < 2; ++c) {
      const int bq = 2 * wr + c;
      if (bq != ho) {
#pragma unroll
        for (int q = 0; q < 4; ++q) {
          f32x4 v;
#pragma unroll
          for (int e = 0; e < 4; ++e) v[e] = fin[c][4 * q + e];
          __builtin_amdgcn_raw_buffer_store_b128(__builtin_bit_cast(u32x4, v), srsrc, gbase + bq * 65536 + ho * 16384 + q * 1024, 0, 16);
        }
      }
    }
    asm volatile("s_waitcnt vmcnt(0)" ::: "memory"); asm volatile("s_barrier" ::: "memory"); SB();
    if (tid == 0) __hip_atomic_store(sy3 + (1 + ho) * 16, tag3, RLX_AGENT);
    ST(37);
    if (wr == (ho >> 1)) {
      poll3(sy3 + 16, tag3, lane, ho);
      if constexpr (USE_ACQ) { __builtin_amdgcn_fence(__ATOMIC_ACQUIRE, "agent"); asm volatile("s_waitcnt vmcnt(0)" ::: "memory"); }
      if (ho == 0 && tid == 0) __hip_atomic_store(sy3, tag3, RLX_AGENT);
      ST(38);
      f32x16 s = (ho & 1) ? fin[1] : fin[0];
      u32x4 ld[3][4];
#pragma unroll
      for (int i = 0; i < 3; ++i) {
        const int src = (ho + 1 + i) & 3;
#pragma unroll
        for (int q = 0; q < 4; ++q) ld[i][q] = __builtin_amdgcn_raw_buffer_load_b128(srsrc, gbase + ho * 65536 + src * 16384 + q * 1024, 0, 16);
      }
#pragma unroll
      for (int i = 0; i < 3; ++i)
#pragma unroll
        for (int q = 0; q < 4; ++q) {
          const f32x4 v = __builtin_bit_cast(f32x4, ld[i][q]);
#pragma unroll
          for (int e = 0; e < 4; ++e) s[4 * q + e] += v[e];
        }
      u16* dst = out + (size_t)(rg * 128 + ho * 32 + r32) * 1024 + br * 512 + hp * 128 + wn * 64 + kh * 32 + 8 * h;
      *(u32x4*)(dst) = pack8(s, 0);
      *(u32x4*)(dst + 16) = pack8(s, 1);
    }
  }
  ST(39);
  ST_OUT(ws, 41);
}

extern "C" void kernel_launch(void* const* d_in, const int* in_sizes, int n_in, void* d_out, int out_size, void* d_ws, size_t ws_size, hipStream_t stream) {
  (void)hipFuncSetAttribute((const void*)fused, hipFuncAttributeMaxDynamicSharedMemorySize, LDS_BYTES);
  fused<<<256, 512, LDS_BYTES, stream>>>((const u16*)d_in[0], (const u16*)d_in[1], (const u16*)d_in[2], (const u16*)d_in[3], (const u16*)d_in[4],
                                         (const u16*)d_in[5], (const u16*)d_in[6], (const u16*)d_in[7], (const u16*)d_in[8], (const u16*)d_in[9],
                                         (char*)d_ws, (u16*)d_out);
}
```

```cpp
#include <hip/hip_runtime.h>
#include <hip/hip_bf16.h>
#include <utility>

typedef unsigned short u16;
typedef unsigned long long u64;
typedef __attribute__((ext_vector_type(4))) short s16x4;
typedef __attribute__((ext_vector_type(8))) short s16x8;
typedef __attribute__((ext_vector_type(8))) __bf16 bf16x8;
typedef __attribute__((ext_vector_type(2))) __bf16 bf16x2;
typedef __attribute__((ext_vector_type(2))) float f32x2;
typedef __attribute__((ext_vector_type(4))) float f32x4;
typedef __attribute__((ext_vector_type(16))) float f32x16;
typedef __attribute__((ext_vector_type(4))) unsigned u32x4;
#define LDS_AS __attribute__((address_space(3)))
#define GAS __attribute__((address_space(1)))
#define DI __device__ __forceinline__
typedef LDS_AS char* lptr;
typedef GAS u64 gu64;
#define RLX_AGENT __ATOMIC_RELAXED, __HIP_MEMORY_SCOPE_AGENT

#define CODE_PF 23552
#define POL_ALL 0
#define WARM_NT 1
#define USE_ACQ 0
#define POLL_V 2

template <int N> DI void wait_v() { asm volatile("s_waitcnt vmcnt(%0)" ::"n"(N) : "memory"); }
#define SB() __builtin_amdgcn_sched_barrier(0)

DI bf16x8 as_bf(s16x8 v) { return __builtin_bit_cast(bf16x8, v); }
DI bf16x8 as_bf(u32x4 v) { return __builtin_bit_cast(bf16x8, v); }
DI unsigned pk2(float a, float b) { bf16x2 r = __builtin_convertvector(f32x2{a, b}, bf16x2); return __builtin_bit_cast(unsigned, r); }
DI float bfr(float a) { bf16x2 r = __builtin_convertvector(f32x2{a, a}, bf16x2); return __uint_as_float(__builtin_bit_cast(unsigned, r) << 16); }
DI f32x16 mfma(bf16x8 a, bf16x8 b, f32x16 c) { return __builtin_amdgcn_mfma_f32_32x32x16_bf16(a, b, c, 0, 0, 0); }
DI s16x4 trread(lptr addr) { return __builtin_amdgcn_ds_read_tr16_b64_v4i16((LDS_AS s16x4*)addr); }
DI bf16x8 trfrag(lptr addr, int off0, int off1) {
  s16x4 lo = trread(addr + off0), hi = trread(addr + off1);
  return as_bf(__builtin_shufflevector(lo, hi, 0, 1, 2, 3, 4, 5, 6, 7));
}
DI bf16x8 ldsfrag(lptr addr) { return *(LDS_AS bf16x8*)addr; }
template <int POL> DI void glds16(unsigned voff, const void* sbase, lptr lds_addr) {
  unsigned keep; const unsigned dst = (unsigned)(size_t)lds_addr;
  if constexpr (POL == 1)
    asm volatile("s_mov_b32 %0, m0\n\ts_mov_b32 m0, %2\n\ts_nop 0\n\tglobal_load_lds_dwordx4 %1, %3 nt\n\ts_mov_b32 m0, %0"
                 : "=&s"(keep) : "v"(voff), "s"(dst), "s"(sbase) : "memory");
  else if constexpr (POL == 2)
    asm volatile("s_mov_b32 %0, m0\n\ts_mov_b32 m0, %2\n\ts_nop 0\n\tglobal_load_lds_dwordx4 %1, %3 sc1\n\ts_mov_b32 m0, %0"
                 : "=&s"(keep) : "v"(voff), "s"(dst), "s"(sbase) : "memory");
  else
    asm volatile("s_mov_b32 %0, m0\n\ts_mov_b32 m0, %2\n\ts_nop 0\n\tglobal_load_lds_dwordx4 %1, %3\n\ts_mov_b32 m0, %0"
                 : "=&s"(keep) : "v"(voff), "s"(dst), "s"(sbase) : "memory");
}
DI u32x4 ldpol(const u32x4* p) { if constexpr (POL_ALL) return __builtin_nontemporal_load(p); else return *p; }
DI u32x4 pack8(const f32x16& a, int s) {
  u32x4 r;
  r[0] = pk2(a[8 * s + 0], a[8 * s + 1]); r[1] = pk2(a[8 * s + 2], a[8 * s + 3]);
  r[2] = pk2(a[8 * s + 4], a[8 * s + 5]); r[3] = pk2(a[8 * s + 6], a[8 * s + 7]);
  return r;
}
template <int... Is, class F> DI void sfor_impl(std::integer_sequence<int, Is...>, F&& f) { (f(std::integral_constant<int, Is>{}), ...); }
template <int N, class F> DI void sfor(F&& f) { sfor_impl(std::make_integer_sequence<int, N>{}, f); }

#define ST_DECL(n)
#define ST(i)
#define ST_OUT(ws, n)

struct Op { int it, pos, tag, piece; };
enum { TX = 100, TW = 200, TKV = 300, TWARM = 400, TWU = 500, TOU = 600 };
struct Sched { Op op[128]; int n; };
#define KV_PRO 16
#define WOW_U 6
constexpr Sched make1() {
  Sched s{}; int n = 0;
  const int pro[6] = {TX + 0, TW + 0, TW + 1, TX + 1, TW + 2, TX + 2};
  for (int i = 0; i < 6; ++i) { s.op[n++] = Op{-1, i / 2, pro[i], 0}; s.op[n++] = Op{-1, i / 2, pro[i], 1}; }
  int kv = 0;
  for (; kv < KV_PRO; ++kv) s.op[n++] = Op{-1, 3, TKV, kv};
  for (int u = 0; u < 16; ++u) {
    const bool hw = u >= 1 && u + 2 < 16, hx = u >= 2 && !(u & 1) && u / 2 + 2 < 8;
    if (hw) s.op[n++] = Op{u, 0, TW + u + 2, 0};
    if (hx) s.op[n++] = Op{u, 1, TX + u / 2 + 2, 0};
    if (kv < 16) s.op[n++] = Op{u, 1, TKV, kv++};
    if (hw) s.op[n++] = Op{u, 2, TW + u + 2, 1};
    if (hx) s.op[n++] = Op{u, 3, TX + u / 2 + 2, 1};
    if (kv < 16) s.op[n++] = Op{u, 3, TKV, kv++};
    if (u == WOW_U) s.op[n++] = Op{u, 3, TWARM, 0};
    if (u == 12) { s.op[n++] = Op{u, 3, TWU + 0, 0}; s.op[n++] = Op{u, 3, TWU + 0, 1}; }
    if (u == 14) { s.op[n++] = Op{u, 0, TWU + 1, 0}; s.op[n++] = Op{u, 1, TWU + 1, 1}; s.op[n++] = Op{u, 2, TWU + 2, 0}; s.op[n++] = Op{u, 3, TWU + 2, 1}; }
    if (u == 15) { s.op[n++] = Op{u, 0, TWU + 3, 0}; s.op[n++] = Op{u, 2, TWU + 3, 1}; }
  }
  s.n = n; return s;
}
constexpr Sched S1 = make1();
constexpr int swait(const Sched& s, int it, int tagA, int tagB) {
  int issued = 0, last = -1;
  for (int i = 0; i < s.n; ++i) if (s.op[i].it < it) { if (s.op[i].tag == tagA || s.op[i].tag == tagB) last = i; issued = i + 1; }
  if (last < 0) return 63;
  const int r = issued - 1 - last; return r > 63 ? 63 : r;
}
constexpr int WSL[16] = {5, 1, 3, 2, 0, 1, 3, 6, 8, 0, 1, 3, 6, 8, 0, 1};
constexpr int OSL[16] = {-1, -1, -1, -1, 4, 5, 2, 7, 9, 4, 5, 2, 7, 9, 4, 5};
constexpr Sched make2() {
  Sched s{}; int n = 0;
  auto add = [&](int v, int pos, int tag) { s.op[n++] = Op{v, pos, tag, 0}; s.op[n++] = Op{v, pos + 1 > 3 ? 3 : pos + 1, tag, 1}; };
  add(2, 0, TOU + 4); add(2, 1, TOU + 5); add(2, 2, TWU + 5);
  add(3, 0, TWU + 6);
  add(4, 0, TOU + 6); add(4, 1, TWU + 7); add(4, 2, TOU + 7); add(4, 3, TWU + 8); add(4, 3, TOU + 8);
  for (int v = 5; v <= 11; ++v) { add(v, 0, TWU + v + 4); add(v, 2, TOU + v + 4); }
  for (int i = 1; i < n; ++i) { Op o = s.op[i]; int j = i - 1; while (j >= 0 && (s.op[j].it > o.it || (s.op[j].it == o.it && s.op[j].pos > o.pos))) { s.op[j + 1] = s.op[j]; --j; } s.op[j + 1] = o; }
  s.n = n; return s;
}
constexpr Sched S2 = make2();

constexpr int LDS_BYTES = 163840;
constexpr size_t WS_SLAB = 16u << 20, WS_SYNC = 32u << 20;

DI bool poll3(gu64* f0, u64 tag, int lane, int skip) {
  for (unsigned spins = 0; spins < (1u << 22); ++spins) {
    u64 v = tag;
    if (lane < 4 && lane != skip) v = __hip_atomic_load(f0 + lane * 16, RLX_AGENT);
    if (__all(v == tag)) return true;
    __builtin_amdgcn_s_sleep(1);
  }
  return false;
}

__global__ __launch_bounds__(512, 2) void fused(const u16* __restrict__ xA, const u16* __restrict__ kA, const u16* __restrict__ vA, const u16* __restrict__ wqA, const u16* __restrict__ woA,
                                                const u16* __restrict__ xB, const u16* __restrict__ kB, const u16* __restrict__ vB, const u16* __restrict__ wqB, const u16* __restrict__ woB,
                                                char* __restrict__ ws, u16* __restrict__ out) {
  extern __shared__ __attribute__((aligned(16))) char smem[];
  const lptr lds0 = (lptr)smem;
  const int id = blockIdx.x, g = id & 7, j = id >> 3;
  const int br = g >> 2, ho = g & 3, rg = j >> 2, hp = j & 3;
  const int tid = threadIdx.x, lane = tid & 63;
  const int w = __builtin_amdgcn_readfirstlane(tid >> 6);
  const u16* x = br ? xB : xA; const u16* kc = br ? kB : kA; const u16* vc = br ? vB : vA; const u16* wq = br ? wqB : wqA; const u16* wo = br ? woB : woA;
  ST_DECL(44); ST(0);
  unsigned cpf = 0;
  {
    const char* pc = (const char*)__builtin_amdgcn_s_getpc();
    const unsigned line = (unsigned)(j * 8 + w) * 64 + lane;
    if (w == 7 - (j & 7) && (unsigned)((j & 3) * 64 + lane) * 128 < CODE_PF)
      asm volatile("global_load_dword %0, %1, %2" : "=v"(cpf) : "v"((unsigned)((j & 3) * 64 + lane) * 128), "s"(pc) : "memory");
    (void)line;
  }
  gu64* sy1 = (gu64*)(ws + WS_SYNC) + (size_t)((br * 4 + ho) * 8 + rg) * 128;
  gu64* sy3 = (gu64*)(ws + WS_SYNC) + (size_t)(64 + (br * 8 + rg) * 4 + hp) * 128;
  const u64 tag1 = __hip_atomic_load(sy1, RLX_AGENT) + 1, tag3 = __hip_atomic_load(sy3, RLX_AGENT) + 1;

  const int r32 = lane & 31, h = lane >> 5;
  const int q4 = (lane & 15) >> 2, p4 = lane & 3, g16 = (lane >> 4) & 1, u4 = ((p4 & 1) << 1) | (p4 >> 1);
  const int head0 = ho * 8 + hp * 2;
  f32x16 acc[4];
  const int kh = w >> 2, wn = (w >> 1) & 1, wr = w & 1, w4 = w & 3;
  unsigned wusrc[2];
#pragma unroll
  for (int i = 0; i < 2; ++i) {
    const int kr = (2 * w4 + i) * 4 + (lane >> 4), c = (lane & 15) ^ ((kr & 3) << 2);
    wusrc[i] = ((kh * 128 + kr) * 512 + 8 * c) * 2;
  }
  const u16* wub = wo + (size_t)(ho * 1024) * 512 + hp * 128;
  const unsigned ousrc = (((w4 * 32 + kh) * 8 * 64 + lane) * 8) * 2;
  const u16* oub = (const u16*)ws + (size_t)((br * 32 + rg * 4) * 32 + ho * 8) * 4096;
  auto ldWU = [&](int v, int i) {
    const int hq = (hp + (v >> 2)) & 3;
    glds16<0>(wusrc[i], wub + (size_t)(hq * 256 + (v & 3) * 32) * 512, lds0 + WSL[v] * 16384 + kh * 8192 + (2 * w4 + i) * 1024);
  };
  auto ldOU = [&](int v, int i) {
    const int hq = (hp + (v >> 2)) & 3;
    glds16<2>(ousrc, oub + (size_t)(hq * 2) * 4096 + ((v & 3) * 2 + i) * 512, lds0 + OSL[v] * 16384 + kh * 8192 + (2 * w4 + i) * 1024);
  };
  {
    const int bi = w >> 1, hi = w & 1;
    const int b = rg * 4 + bi, head = head0 + hi;
    unsigned xsrc[2], wsrc[2];
#pragma unroll
    for (int i = 0; i < 2; ++i) {
      const int row = (2 * w + i) * 8 + (lane >> 3), c = (lane & 7) ^ ((row >> 1) & 7);
      xsrc[i] = (row * 512 + 8 * c) * 2;
      const int kr = (2 * w + i) * 2 + (lane >> 5), c2 = (lane & 31) ^ ((kr & 3) << 2);
      wsrc[i] = (kr * 4096 + 8 * c2) * 2;
    }
    const u16* xb0 = x + (size_t)rg * 128 * 512;
    const u16* wb0 = wq + head0 * 128;
    const unsigned wowsrc = ((w * 16 + (lane >> 2)) * 512 + (lane & 3) * 32) * 2;
    unsigned wowarm = 0;
    const u16* kbase = kc + (size_t)(b * 32 + head) * 4096 + r32 * 128 + 8 * h;
    const u16* vbase = vc + (size_t)(b * 32 + head) * 4096;
    const unsigned vsrc = ((lane >> 4) * 128 + 8 * ((lane & 15) ^ ((lane >> 4) << 2))) * 2;
    const lptr varea = lds0 + 6 * 16384 + w * 8192;
    bf16x8 kf[8];
    auto issue = [&](auto IT, auto POS) {
      sfor<S1.n>([&](auto I) {
        constexpr Op o = S1.op[I];
        if constexpr (o.it == IT && o.pos == POS) {
          if constexpr (o.tag >= TWU) ldWU(o.tag - TWU, o.piece);
          else if constexpr (o.tag >= TWARM) asm volatile("global_load_dword %0, %1, %2" : "=v"(wowarm) : "v"(wowsrc), "s"(wub + (size_t)(rg * 128) * 512) : "memory");
          else if constexpr (o.tag >= TKV) {
            if constexpr (o.piece < 8) glds16<0>(vsrc, vbase + o.piece * 512, varea + o.piece * 1024);
            else kf[o.piece - 8] = *(const bf16x8*)(kbase + 16 * (o.piece - 8));
          } else if constexpr (o.tag >= TW) glds16<0>(wsrc[o.piece], wb0 + (size_t)32 * (o.tag - TW) * 4096, lds0 + ((o.tag - TW) % 3) * 16384 + (2 * w + o.piece) * 1024);
          else glds16<0>(xsrc[o.piece], xb0 + 64 * (o.tag - TX), lds0 + (3 + (o.tag - TX) % 3) * 16384 + (2 * w + o.piece) * 1024);
        }
      });
    };
    sfor<4>([&](auto G) { issue(std::integral_constant<int, -1>{}, G); if constexpr (G < 3) asm volatile("s_barrier" ::: "memory"); });

    const int xrow = bi * 32 + r32;
    const unsigned xoff = 3 * 16384 + xrow * 128 + ((h ^ ((xrow >> 1) & 7)) << 4);
    const unsigned woff = (8 * h + q4) * 512 + (4 * hi + q4) * 64 + g16 * 32 + u4 * 8;
#pragma unroll
    for (int t = 0; t < 4; ++t) acc[t] = f32x16{};
    bf16x8 xb[2], wa[2][4];
    auto rdfrags = [&](int kk) {
      const int u = kk >> 1, ks = kk & 1, X = kk >> 2, ks4 = kk & 3, buf = kk & 1;
      xb[buf] = ldsfrag(lds0 + (X % 3) * 16384 + (xoff ^ (ks4 << 5)));
#pragma unroll
      for (int t = 0; t < 4; ++t) wa[buf][t] = trfrag(lds0 + (u % 3) * 16384 + (woff ^ (t << 6)), ks * 8192, ks * 8192 + 2048);
    };
    sfor<16>([&](auto U) {
      constexpr int u = U;
      SB(); wait_v<swait(S1, u, TW + u, TX + u / 2)>(); asm volatile("s_barrier" ::: "memory"); SB();
      ST(1 + u);
      rdfrags(2 * u); rdfrags(2 * u + 1);
      issue(U, std::integral_constant<int, 0>{});
      acc[0] = mfma(wa[0][0], xb[0], acc[0]); acc[1] = mfma(wa[0][1], xb[0], acc[1]);
      issue(U, std::integral_constant<int, 1>{});
      acc[2] = mfma(wa[0][2], xb[0], acc[2]); acc[3] = mfma(wa[0][3], xb[0], acc[3]);
      issue(U, std::integral_constant<int, 2>{});
      acc[0] = mfma(wa[1][0], xb[1], acc[0]); acc[1] = mfma(wa[1][1], xb[1], acc[1]);
      issue(U, std::integral_constant<int, 3>{});
      acc[2] = mfma(wa[1][2], xb[1], acc[2]); acc[3] = mfma(wa[1][3], xb[1], acc[3]);
    });
    asm volatile("" :: "v"(cpf));
    asm volatile("" :: "v"(wowarm));
    SB(); asm volatile("s_barrier" ::: "memory"); SB();
    ST(17);
    ldWU(4, 0); ldWU(4, 1);
    f32x16 sc = f32x16{};
#pragma unroll
    for (int t = 0; t < 4; ++t)
#pragma unroll
      for (int s2 = 0; s2 < 2; ++s2) sc = mfma(kf[2 * t + s2], as_bf(pack8(acc[t], s2)), sc);
    constexpr float SCALE = 0.08838834764831845f, LOG2E = 1.4426950408889634f;
    float m = -3.0e38f;
#pragma unroll
    for (int i = 0; i < 16; ++i) { sc[i] = bfr(bfr(sc[i]) * SCALE); m = fmaxf(m, sc[i]); }
    m = fmaxf(m, __shfl_xor(m, 32));
    float sum = 0.f;
#pragma unroll
    for (int i = 0; i < 16; ++i) { sc[i] = __builtin_amdgcn_exp2f((sc[i] - m) * LOG2E); sum += sc[i]; }
    sum += __shfl_xor(sum, 32);
    const float inv = 1.0f / sum;
#pragma unroll
    for (int i = 0; i < 16; ++i) sc[i] *= inv;
    const bf16x8 pf0 = as_bf(pack8(sc, 0)), pf1 = as_bf(pack8(sc, 1));
    const unsigned voff = (4 * h + q4) * 256 + q4 * 64 + g16 * 32 + u4 * 8;
    const __amdgpu_buffer_rsrc_t orsrc = __builtin_amdgcn_make_buffer_rsrc((void*)ws, 0, 16 << 20, 0x00020000);
    const unsigned ooff = (((br * 32 + b) * 32 + head) * 8 * 64 + lane) * 16;
#pragma unroll
    for (int t = 0; t < 4; ++t) {
      acc[t] = f32x16{};
      acc[t] = mfma(trfrag(varea + (voff ^ (t << 6)), 0, 2048), pf0, acc[t]);
      acc[t] = mfma(trfrag(varea + (voff ^ (t << 6)), 4096, 4096 + 2048), pf1, acc[t]);
    }
#pragma unroll
    for (int t = 0; t < 4; ++t) {
      const u32x4 o0 = pack8(acc[t], 0), o1 = pack8(acc[t], 1);
      __builtin_amdgcn_raw_buffer_store_b128(o0, orsrc, ooff + (2 * t) * 1024, 0, 16);
      __builtin_amdgcn_raw_buffer_store_b128(o1, orsrc, ooff + (2 * t + 1) * 1024, 0, 16);
      *(LDS_AS u32x4*)(varea + (2 * t) * 1024 + lane * 16) = o0;
      *(LDS_AS u32x4*)(varea + (2 * t + 1) * 1024 + lane * 16) = o1;
    }
  }
  ST(18);
  f32x16 ac2[2][2];
  {
#pragma unroll
    for (int a = 0; a < 2; ++a)
#pragma unroll
      for (int c = 0; c < 2; ++c) ac2[a][c] = f32x16{};
    const unsigned woff = kh * 8192 + (8 * h + q4) * 256 + ((2 * wn) ^ q4) * 64 + g16 * 32 + u4 * 8;
    const unsigned ooff = kh * 8192 + (wr * 2) * 2048 + lane * 16;
    const lptr own0 = lds0 + (6 + 2 * wr) * 16384 + kh * 8192 + lane * 16, own1 = own0 + 16384;
    auto issue2 = [&](auto V, auto POS) {
      sfor<S2.n>([&](auto I) {
        constexpr Op o = S2.op[I];
        if constexpr (o.it == V && o.pos == POS) {
          if constexpr (o.tag >= TOU) ldOU(o.tag - TOU, o.piece); else ldWU(o.tag - TWU, o.piece);
        }
      });
    };
    sfor<16>([&](auto V) {
      constexpr int v = V;
      if constexpr (v == 0) {
        SB(); asm volatile("s_waitcnt vmcnt(0) lgkmcnt(0)" ::: "memory"); asm volatile("s_barrier" ::: "memory"); SB();
        if (tid == 0) __hip_atomic_store(sy1 + (1 + hp) * 16, tag1, RLX_AGENT);
      } else {
        if constexpr (v == POLL_V) {
          if (w == 0) {
            poll3(sy1 + 16, tag1, lane, hp);
            if constexpr (USE_ACQ) { __builtin_amdgcn_fence(__ATOMIC_ACQUIRE, "agent"); asm volatile("s_waitcnt vmcnt(0)" ::: "memory"); }
            if (hp == 0 && lane == 0) __hip_atomic_store(sy1, tag1, RLX_AGENT);
          }
        }
        SB(); wait_v<swait(S2, v, TWU + v, TOU + v)>(); asm volatile("s_barrier" ::: "memory"); SB();
      }
      ST(19 + v);
      sfor<2>([&](auto KS) {
        constexpr int ks = KS;
        bf16x8 wa[2], ob[2];
        const lptr wbase = lds0 + WSL[v] * 16384;
#pragma unroll
        for (int t = 0; t < 2; ++t) wa[t] = trfrag(wbase + (woff ^ (t << 6)), ks * 4096, ks * 4096 + 1024);
        if constexpr (v < 4) { ob[0] = ldsfrag(own0 + (2 * v + ks) * 1024); ob[1] = ldsfrag(own1 + (2 * v + ks) * 1024); }
        else {
#pragma unroll
          for (int t = 0; t < 2; ++t) ob[t] = ldsfrag(lds0 + OSL[v] * 16384 + ooff + t * 2048 + ks * 1024);
        }
        issue2(V, std::integral_constant<int, 2 * ks>{});
        ac2[0][0] = mfma(wa[0], ob[0], ac2[0][0]);
        ac2[0][1] = mfma(wa[0], ob[1], ac2[0][1]);
        issue2(V, std::integral_constant<int, 2 * ks + 1>{});
        ac2[1][0] = mfma(wa[1], ob[0], ac2[1][0]);
        ac2[1][1] = mfma(wa[1], ob[1], ac2[1][1]);
      });
      SB();
    });
  }
  ST(35);
  f32x16 fin[2];
  {
    const lptr my = lds0 + 6 * 16384 + w * 8192 + lane * 16, other = lds0 + 6 * 16384 + (w ^ 4) * 8192 + lane * 16;
#pragma unroll
    for (int c = 0; c < 2; ++c)
#pragma unroll
      for (int q = 0; q < 4; ++q) {
        f32x4 v;
#pragma unroll
        for (int e = 0; e < 4; ++e) v[e] = kh ? ac2[0][c][4 * q + e] : ac2[1][c][4 * q + e];
        *(LDS_AS f32x4*)(my + (c * 4 + q) * 1024) = v;
      }
    asm volatile("s_waitcnt lgkmcnt(0)" ::: "memory"); asm volatile("s_barrier" ::: "memory"); SB();
#pragma unroll
    for (int c = 0; c < 2; ++c)
#pragma unroll
      for (int q = 0; q < 4; ++q) {
        f32x4 v = *(LDS_AS f32x4*)(other + (c * 4 + q) * 1024);
#pragma unroll
        for (int e = 0; e < 4; ++e) fin[c][4 * q + e] = v[e] + (kh ? ac2[1][c][4 * q + e] : ac2[0][c][4 * q + e]);
      }
  }
  ST(36);
  {
    const __amdgpu_buffer_rsrc_t srsrc = __builtin_amdgcn_make_buffer_rsrc((void*)(ws + WS_SLAB), 0, 16 << 20, 0x00020000);
    const unsigned gbase = (unsigned)((br * 8 + rg) * 4 + hp) * 262144u + (wn * 2 + kh) * 4096 + lane * 16;
#pragma unroll
    for (int c = 0; c < 2; ++c) {
      const int bq = 2 * wr + c;
      if (bq != ho) {
#pragma unroll
        for (int q = 0; q < 4; ++q) {
          f32x4 v;
#pragma unroll
          for (int e = 0; e < 4; ++e) v[e] = fin[c][4 * q + e];
          __builtin_amdgcn_raw_buffer_store_b128(__builtin_bit_cast(u32x4, v), srsrc, gbase + bq * 65536 + ho * 16384 + q * 1024, 0, 16);
        }
      }
    }
    asm volatile("s_waitcnt vmcnt(0)" ::: "memory"); asm volatile("s_barrier" ::: "memory"); SB();
    if (tid == 0) __hip_atomic_store(sy3 + (1 + ho) * 16, tag3, RLX_AGENT);
    ST(37);
    if (wr == (ho >> 1)) {
      poll3(sy3 + 16, tag3, lane, ho);
      if constexpr (USE_ACQ) { __builtin_amdgcn_fence(__ATOMIC_ACQUIRE, "agent"); asm volatile("s_waitcnt vmcnt(0)" ::: "memory"); }
      if (ho == 0 && tid == 0) __hip_atomic_store(sy3, tag3, RLX_AGENT);
      ST(38);
      f32x16 s = (ho & 1) ? fin[1] : fin[0];
      u32x4 ld[3][4];
#pragma unroll
      for (int i = 0; i < 3; ++i) {
        const int src = (ho + 1 + i) & 3;
#pragma unroll
        for (int q = 0; q < 4; ++q) ld[i][q] = __builtin_amdgcn_raw_buffer_load_b128(srsrc, gbase + ho * 65536 + src * 16384 + q * 1024, 0, 16);
      }
#pragma unroll
      for (int i = 0; i < 3; ++i)
#pragma unroll
        for (int q = 0; q < 4; ++q) {
          const f32x4 v = __builtin_bit_cast(f32x4, ld[i][q]);
#pragma unroll
          for (int e = 0; e < 4; ++e) s[4 * q + e] += v[e];
        }
      u16* dst = out + (size_t)(rg * 128 + ho * 32 + r32) * 1024 + br * 512 + hp * 128 + wn * 64 + kh * 32 + 8 * h;
      *(u32x4*)(dst) = pack8(s, 0);
      *(u32x4*)(dst + 16) = pack8(s, 1);
    }
  }
  ST(39);
  ST_OUT(ws, 41);
}

extern "C" void kernel_launch(void* const* d_in, const int* in_sizes, int n_in, void* d_out, int out_size, void* d_ws, size_t ws_size, hipStream_t stream) {
  (void)hipFuncSetAttribute((const void*)fused, hipFuncAttributeMaxDynamicSharedMemorySize, LDS_BYTES);
  fused<<<256, 512, LDS_BYTES, stream>>>((const u16*)d_in[0], (const u16*)d_in[1], (const u16*)d_in[2], (const u16*)d_in[3], (const u16*)d_in[4],
                                         (const u16*)d_in[5], (const u16*)d_in[6], (const u16*)d_in[7], (const u16*)d_in[8], (const u16*)d_in[9],
                                         (char*)d_ws, (u16*)d_out);
}
```

```cpp
#include <hip/hip_runtime.h>
#include <hip/hip_bf16.h>
#include <utility>

typedef unsigned short u16;
typedef unsigned long long u64;
typedef __attribute__((ext_vector_type(4))) short s16x4;
typedef __attribute__((ext_vector_type(8))) short s16x8;
typedef __attribute__((ext_vector_type(8))) __bf16 bf16x8;
typedef __attribute__((ext_vector_type(2))) __bf16 bf16x2;
typedef __attribute__((ext_vector_type(2))) float f32x2;
typedef __attribute__((ext_vector_type(4))) float f32x4;
typedef __attribute__((ext_vector_type(16))) float f32x16;
typedef __attribute__((ext_vector_type(4))) unsigned u32x4;
#define LDS_AS __attribute__((address_space(3)))
#define GAS __attribute__((address_space(1)))
#define DI __device__ __forceinline__
typedef LDS_AS char* lptr;
typedef GAS u64 gu64;
#define RLX_AGENT __ATOMIC_RELAXED, __HIP_MEMORY_SCOPE_AGENT

#define CODE_PF 25088
#define POL_ALL 0
#define WARM_NT 1
#define USE_ACQ 0
#define POLL_V 2

template <int N> DI void wait_v() { asm volatile("s_waitcnt vmcnt(%0)" ::"n"(N) : "memory"); }
#define SB() __builtin_amdgcn_sched_barrier(0)

DI bf16x8 as_bf(s16x8 v) { return __builtin_bit_cast(bf16x8, v); }
DI bf16x8 as_bf(u32x4 v) { return __builtin_bit_cast(bf16x8, v); }
DI unsigned pk2(float a, float b) { bf16x2 r = __builtin_convertvector(f32x2{a, b}, bf16x2); return __builtin_bit_cast(unsigned, r); }
DI float bfr(float a) { bf16x2 r = __builtin_convertvector(f32x2{a, a}, bf16x2); return __uint_as_float(__builtin_bit_cast(unsigned, r) << 16); }
DI f32x16 mfma(bf16x8 a, bf16x8 b, f32x16 c) { return __builtin_amdgcn_mfma_f32_32x32x16_bf16(a, b, c, 0, 0, 0); }
DI s16x4 trread(lptr addr) { return __builtin_amdgcn_ds_read_tr16_b64_v4i16((LDS_AS s16x4*)addr); }
DI bf16x8 trfrag(lptr addr, int off0, int off1) {
  s16x4 lo = trread(addr + off0), hi = trread(addr + off1);
  return as_bf(__builtin_shufflevector(lo, hi, 0, 1, 2, 3, 4, 5, 6, 7));
}
DI bf16x8 ldsfrag(lptr addr) { return *(LDS_AS bf16x8*)addr; }
template <int POL> DI void glds16(unsigned voff, const void* sbase, lptr lds_addr) {
  unsigned keep; const unsigned dst = (unsigned)(size_t)lds_addr;
  if constexpr (POL == 1)
    asm volatile("s_mov_b32 %0, m0\n\ts_mov_b32 m0, %2\n\ts_nop 0\n\tglobal_load_lds_dwordx4 %1, %3 nt\n\ts_mov_b32 m0, %0"
                 : "=&s"(keep) : "v"(voff), "s"(dst), "s"(sbase) : "memory");
  else if constexpr (POL == 2)
    asm volatile("s_mov_b32 %0, m0\n\ts_mov_b32 m0, %2\n\ts_nop 0\n\tglobal_load_lds_dwordx4 %1, %3 sc1\n\ts_mov_b32 m0, %0"
                 : "=&s"(keep) : "v"(voff), "s"(dst), "s"(sbase) : "memory");
  else
    asm volatile("s_mov_b32 %0, m0\n\ts_mov_b32 m0, %2\n\ts_nop 0\n\tglobal_load_lds_dwordx4 %1, %3\n\ts_mov_b32 m0, %0"
                 : "=&s"(keep) : "v"(voff), "s"(dst), "s"(sbase) : "memory");
}
DI u32x4 ldpol(const u32x4* p) { return __builtin_nontemporal_load(p); }
DI u32x4 pack8(const f32x16& a, int s) {
  u32x4 r;
  r[0] = pk2(a[8 * s + 0], a[8 * s + 1]); r[1] = pk2(a[8 * s + 2], a[8 * s + 3]);
  r[2] = pk2(a[8 * s + 4], a[8 * s + 5]); r[3] = pk2(a[8 * s + 6], a[8 * s + 7]);
  return r;
}
template <int... Is, class F> DI void sfor_impl(std::integer_sequence<int, Is...>, F&& f) { (f(std::integral_constant<int, Is>{}), ...); }
template <int N, class F> DI void sfor(F&& f) { sfor_impl(std::make_integer_sequence<int, N>{}, f); }

#define ST_DECL(n)
#define ST(i)
#define ST_OUT(ws, n)

struct Op { int it, pos, tag, piece; };
enum { TX = 100, TW = 200, TKV = 300, TWARM = 400, TWU = 500, TOU = 600 };
struct Sched { Op op[128]; int n; };
constexpr Sched make1() {
  Sched s{}; int n = 0;
  const int pro[9] = {TX + 0, TW + 0, TW + 1, TX + 1, TW + 2, TW + 3, TX + 2, TW + 4, TW + 5};
  for (int i = 0; i < 9; ++i) { s.op[n++] = Op{-1, 0, pro[i], 0}; s.op[n++] = Op{-1, 0, pro[i], 1}; }
  for (int u = 0; u < 16; ++u) {
    const bool hw = u >= 1 && u <= 10, hx = u >= 2 && u <= 10 && !(u & 1), hkv = u <= 7, hwm = u >= 2 && u <= 5;
    if (hw) s.op[n++] = Op{u, 0, TW + u + 5, 0};
    if (hx) s.op[n++] = Op{u, 1, TX + u / 2 + 2, 0};
    if (hkv) s.op[n++] = Op{u, 1, TKV, 2 * u};
    if (hw) s.op[n++] = Op{u, 2, TW + u + 5, 1};
    if (hx) s.op[n++] = Op{u, 3, TX + u / 2 + 2, 1};
    if (hkv) s.op[n++] = Op{u, 3, TKV, 2 * u + 1};
    if (hwm) s.op[n++] = Op{u, 3, TWARM, u - 2};
  }
  s.n = n; return s;
}
constexpr Sched S1 = make1();
constexpr int swait(const Sched& s, int it, int tagA, int tagB) {
  int issued = 0, last = -1;
  for (int i = 0; i < s.n; ++i) if (s.op[i].it < it) { if (s.op[i].tag == tagA || s.op[i].tag == tagB) last = i; issued = i + 1; }
  if (last < 0) return 63;
  const int r = issued - 1 - last; return r > 63 ? 63 : r;
}
constexpr int WSL[16] = {3, 7, 0, 1, 6, 3, 0, 9, 5, 6, 3, 0, 9, 5, 6, 3};
constexpr int OSL[16] = {-1, -1, -1, -1, 2, 7, 1, 4, 8, 2, 7, 1, 4, 8, 2, 7};
constexpr Sched make2() {
  Sched s{}; int n = 0;
  auto add = [&](int v, int pos, int tag) { s.op[n++] = Op{v, pos, tag, 0}; s.op[n++] = Op{v, pos + 1 > 3 ? 3 : pos + 1, tag, 1}; };
  add(0, 0, TWU + 2); add(0, 2, TWU + 3); add(0, 3, TWU + 4);
  add(1, 0, TWU + 5);
  add(2, 0, TOU + 4); add(2, 2, TOU + 5);
  add(3, 0, TWU + 6);
  add(4, 0, TOU + 6); add(4, 1, TWU + 7); add(4, 2, TOU + 7); add(4, 3, TWU + 8); add(4, 3, TOU + 8);
  for (int v = 5; v <= 11; ++v) { add(v, 0, TWU + v + 4); add(v, 2, TOU + v + 4); }
  for (int i = 1; i < n; ++i) { Op o = s.op[i]; int j = i - 1; while (j >= 0 && (s.op[j].it > o.it || (s.op[j].it == o.it && s.op[j].pos > o.pos))) { s.op[j + 1] = s.op[j]; --j; } s.op[j + 1] = o; }
  s.n = n; return s;
}
constexpr Sched S2 = make2();

constexpr int LDS_BYTES = 163840;
constexpr size_t WS_SLAB = 16u << 20, WS_SYNC = 32u << 20;

DI bool poll3(gu64* f0, u64 tag, int lane, int skip) {
  for (unsigned spins = 0; spins < (1u << 22); ++spins) {
    u64 v = tag;
    if (lane < 4 && lane != skip) v = __hip_atomic_load(f0 + lane * 16, RLX_AGENT);
    if (__all(v == tag)) return true;
    __builtin_amdgcn_s_sleep(1);
  }
  return false;
}

__global__ __launch_bounds__(512, 2) void fused(const u16* __restrict__ xA, const u16* __restrict__ kA, const u16* __restrict__ vA, const u16* __restrict__ wqA, const u16* __restrict__ woA,
                                                const u16* __restrict__ xB, const u16* __restrict__ kB, const u16* __restrict__ vB, const u16* __restrict__ wqB, const u16* __restrict__ woB,
                                                char* __restrict__ ws, u16* __restrict__ out) {
  extern __shared__ __attribute__((aligned(16))) char smem[];
  const lptr lds0 = (lptr)smem;
  const int id = blockIdx.x, g = id & 7, j = id >> 3;
  const int br = g >> 2, ho = g & 3, rg = j >> 2, hp = j & 3;
  const int tid = threadIdx.x, lane = tid & 63;
  const int w = __builtin_amdgcn_readfirstlane(tid >> 6);
  const u16* x = br ? xB : xA; const u16* kc = br ? kB : kA; const u16* vc = br ? vB : vA; const u16* wq = br ? wqB : wqA; const u16* wo = br ? woB : woA;
  ST_DECL(44); ST(0);
  unsigned cpf = 0;
  {
    const char* pc = (const char*)__builtin_amdgcn_s_getpc();
    const unsigned line = (unsigned)(j * 8 + w) * 64 + lane;
    if (w == 7 - (j & 7) && (unsigned)((j & 3) * 64 + lane) * 128 < CODE_PF)
      asm volatile("global_load_dword %0, %1, %2" : "=v"(cpf) : "v"((unsigned)((j & 3) * 64 + lane) * 128), "s"(pc) : "memory");
    (void)line;
  }
  gu64* sy1 = (gu64*)(ws + WS_SYNC) + (size_t)((br * 4 + ho) * 8 + rg) * 128;
  gu64* sy3 = (gu64*)(ws + WS_SYNC) + (size_t)(64 + (br * 8 + rg) * 4 + hp) * 128;
  const u64 tag1 = __hip_atomic_load(sy1, RLX_AGENT) + 1, tag3 = __hip_atomic_load(sy3, RLX_AGENT) + 1;

  const int r32 = lane & 31, h = lane >> 5;
  const int q4 = (lane & 15) >> 2, p4 = lane & 3, g16 = (lane >> 4) & 1, u4 = ((p4 & 1) << 1) | (p4 >> 1);
  const int head0 = ho * 8 + hp * 2;
  f32x16 acc[4];
  {
    const int bi = w >> 1, hi = w & 1;
    const int b = rg * 4 + bi, head = head0 + hi;
    unsigned xsrc[2], wsrc[2];
#pragma unroll
    for (int i = 0; i < 2; ++i) {
      const int row = (2 * w + i) * 8 + (lane >> 3), c = (lane & 7) ^ ((row >> 1) & 7);
      xsrc[i] = (row * 512 + 8 * c) * 2;
      const int kr = (2 * w + i) * 2 + (lane >> 5), c2 = (lane & 31) ^ ((kr & 3) << 2);
      wsrc[i] = (kr * 4096 + 8 * c2) * 2;
    }
    const u16* xb0 = x + (size_t)rg * 128 * 512;
    const u16* wb0 = wq + head0 * 128;
    const unsigned warmsrc = (((w * 16 + (lane >> 4)) * 512) + (lane & 15) * 8) * 2;
    const u16* warmb = wo + (size_t)(ho * 1024 + rg * 128) * 512 + hp * 128;
    const u16* kbase = kc + (size_t)(b * 32 + head) * 4096 + lane * 8;
    const u16* vbase = vc + (size_t)(b * 32 + head) * 4096 + lane * 8;
    u32x4 kv[16];
    auto issue = [&](auto IT, auto POS) {
      sfor<S1.n>([&](auto I) {
        constexpr Op o = S1.op[I];
        if constexpr (o.it == IT && o.pos == POS) {
          if constexpr (o.tag >= TWARM) glds16<POL_ALL>(warmsrc, warmb + o.piece * 4 * 512, lds0 + 9 * 16384 + w * 2048 + (o.piece & 1) * 1024);
          else if constexpr (o.tag >= TKV) {
            if constexpr (o.piece < 8) kv[o.piece] = ldpol((const u32x4*)(kbase + o.piece * 512));
            else kv[o.piece] = ldpol((const u32x4*)(vbase + (o.piece - 8) * 512));
          } else if constexpr (o.tag >= TW) glds16<POL_ALL>(wsrc[o.piece], wb0 + (size_t)32 * (o.tag - TW) * 4096, lds0 + ((o.tag - TW) % 6) * 16384 + (2 * w + o.piece) * 1024);
          else glds16<POL_ALL>(xsrc[o.piece], xb0 + 64 * (o.tag - TX), lds0 + (6 + (o.tag - TX) % 3) * 16384 + (2 * w + o.piece) * 1024);
        }
      });
    };
    issue(std::integral_constant<int, -1>{}, std::integral_constant<int, 0>{});

    const int xrow = bi * 32 + r32;
    const unsigned xoff = 6 * 16384 + xrow * 128 + ((h ^ ((xrow >> 1) & 7)) << 4);
    const unsigned woff = (8 * h + q4) * 512 + (4 * hi + q4) * 64 + g16 * 32 + u4 * 8;
    const int kslot = (bi == 0) ? 9 : (bi == 1) ? 4 : (bi == 2) ? 5 : 8, vslot = (bi == 0) ? 0 : (bi == 1) ? 1 : (bi == 2) ? 6 : 2;
    const lptr karea = lds0 + kslot * 16384 + hi * 8192, varea = lds0 + vslot * 16384 + hi * 8192;
#pragma unroll
    for (int t = 0; t < 4; ++t) acc[t] = f32x16{};
    bf16x8 xb[2], wa[2][4];
    auto rdfrags = [&](int kk) {
      const int u = kk >> 1, ks = kk & 1, X = kk >> 2, ks4 = kk & 3, buf = kk & 1;
      xb[buf] = ldsfrag(lds0 + (X % 3) * 16384 + (xoff ^ (ks4 << 5)));
#pragma unroll
      for (int t = 0; t < 4; ++t) wa[buf][t] = trfrag(lds0 + (u % 6) * 16384 + (woff ^ (t << 6)), ks * 8192, ks * 8192 + 2048);
    };
    auto parkK = [&]() {
#pragma unroll
      for (int i = 0; i < 8; ++i) { const int row = 4 * i + (lane >> 4); *(LDS_AS u32x4*)(karea + row * 256 + (((lane & 15) ^ (row & 15)) << 4)) = kv[i]; }
    };
    auto parkV = [&]() {
#pragma unroll
      for (int i = 0; i < 8; ++i) { const int row = 4 * i + (lane >> 4); *(LDS_AS u32x4*)(varea + row * 256 + (((lane & 15) ^ ((row & 3) << 2)) << 4)) = kv[8 + i]; }
    };
    sfor<16>([&](auto U) {
      constexpr int u = U;
      SB(); wait_v<swait(S1, u, TW + u + 1, TX + (u + 1) / 2)>(); asm volatile("s_barrier" ::: "memory"); SB();
      ST(1 + u);
      if constexpr (u == 0) rdfrags(0);
      rdfrags(2 * u + 1);
      issue(U, std::integral_constant<int, 0>{});
      acc[0] = mfma(wa[0][0], xb[0], acc[0]); acc[1] = mfma(wa[0][1], xb[0], acc[1]);
      issue(U, std::integral_constant<int, 1>{});
      acc[2] = mfma(wa[0][2], xb[0], acc[2]); acc[3] = mfma(wa[0][3], xb[0], acc[3]);
      SB();
      if constexpr (u < 15) rdfrags(2 * u + 2);
      issue(U, std::integral_constant<int, 2>{});
      acc[0] = mfma(wa[1][0], xb[1], acc[0]); acc[1] = mfma(wa[1][1], xb[1], acc[1]);
      issue(U, std::integral_constant<int, 3>{});
      acc[2] = mfma(wa[1][2], xb[1], acc[2]); acc[3] = mfma(wa[1][3], xb[1], acc[3]);
      SB();
      if constexpr (u == 10) { if (bi == 0) parkK(); }
      if constexpr (u == 11) { if (bi == 1) parkK(); }
      if constexpr (u == 12) { if (bi >= 2) parkK(); }
      if constexpr (u == 13) { if (bi == 0) parkV(); }
      if constexpr (u == 14) { if (bi == 1 || bi == 2) parkV(); }
      if constexpr (u == 15) { if (bi == 3) parkV(); }
    });
    asm volatile("" :: "v"(cpf));
    SB(); asm volatile("s_barrier" ::: "memory"); SB();
    ST(17);
  }
  const int kh = w >> 2, wn = (w >> 1) & 1, wr = w & 1, w4 = w & 3;
  unsigned wusrc[2];
#pragma unroll
  for (int i = 0; i < 2; ++i) {
    const int kr = (2 * w4 + i) * 4 + (lane >> 4), c = (lane & 15) ^ ((kr & 3) << 2);
    wusrc[i] = ((kh * 128 + kr) * 512 + 8 * c) * 2;
  }
  const u16* wub = wo + (size_t)(ho * 1024) * 512 + hp * 128;
  const unsigned ousrc = (((w4 * 32 + kh) * 8 * 64 + lane) * 8) * 2;
  const u16* oub = (const u16*)ws + (size_t)((br * 32 + rg * 4) * 32 + ho * 8) * 4096;
  auto ldWU = [&](int v, int i) {
    const int hq = (hp + (v >> 2)) & 3;
    glds16<POL_ALL>(wusrc[i], wub + (size_t)(hq * 256 + (v & 3) * 32) * 512, lds0 + WSL[v] * 16384 + kh * 8192 + (2 * w4 + i) * 1024);
  };
  auto ldOU = [&](int v, int i) {
    const int hq = (hp + (v >> 2)) & 3;
    glds16<2>(ousrc, oub + (size_t)(hq * 2) * 4096 + ((v & 3) * 2 + i) * 512, lds0 + OSL[v] * 16384 + kh * 8192 + (2 * w4 + i) * 1024);
  };
  ldWU(0, 0); ldWU(0, 1); ldWU(1, 0); ldWU(1, 1);
  {
    const int bi = w >> 1, hi = w & 1;
    const int b = rg * 4 + bi, head = head0 + hi;
    const int kslot = (bi == 0) ? 9 : (bi == 1) ? 4 : (bi == 2) ? 5 : 8, vslot = (bi == 0) ? 0 : (bi == 1) ? 1 : (bi == 2) ? 6 : 2;
    const lptr karea = lds0 + kslot * 16384 + hi * 8192, varea = lds0 + vslot * 16384 + hi * 8192;
    const unsigned koff = r32 * 256 + ((h ^ (r32 & 15)) << 4);
    f32x16 sc = f32x16{};
#pragma unroll
    for (int t = 0; t < 4; ++t)
#pragma unroll
      for (int s2 = 0; s2 < 2; ++s2) sc = mfma(ldsfrag(karea + (koff ^ ((2 * t + s2) << 5))), as_bf(pack8(acc[t], s2)), sc);
    constexpr float SCALE = 0.08838834764831845f, LOG2E = 1.4426950408889634f;
    float m = -3.0e38f;
#pragma unroll
    for (int i = 0; i < 16; ++i) { sc[i] = bfr(bfr(sc[i]) * SCALE); m = fmaxf(m, sc[i]); }
    m = fmaxf(m, __shfl_xor(m, 32));
    float sum = 0.f;
#pragma unroll
    for (int i = 0; i < 16; ++i) { sc[i] = __builtin_amdgcn_exp2f((sc[i] - m) * LOG2E); sum += sc[i]; }
    sum += __shfl_xor(sum, 32);
    const float inv = 1.0f / sum;
#pragma unroll
    for (int i = 0; i < 16; ++i) sc[i] *= inv;
    const bf16x8 pf0 = as_bf(pack8(sc, 0)), pf1 = as_bf(pack8(sc, 1));
    const unsigned voff = (4 * h + q4) * 256 + q4 * 64 + g16 * 32 + u4 * 8;
    const __amdgpu_buffer_rsrc_t orsrc = __builtin_amdgcn_make_buffer_rsrc((void*)ws, 0, 16 << 20, 0x00020000);
    const unsigned ooff = (((br * 32 + b) * 32 + head) * 8 * 64 + lane) * 16;
#pragma unroll
    for (int t = 0; t < 4; ++t) {
      f32x16 o = f32x16{};
      o = mfma(trfrag(varea + (voff ^ (t << 6)), 0, 2048), pf0, o);
      o = mfma(trfrag(varea + (voff ^ (t << 6)), 4096, 4096 + 2048), pf1, o);
      const u32x4 o0 = pack8(o, 0), o1 = pack8(o, 1);
      __builtin_amdgcn_raw_buffer_store_b128(o0, orsrc, ooff + (2 * t) * 1024, 0, 16);
      __builtin_amdgcn_raw_buffer_store_b128(o1, orsrc, ooff + (2 * t + 1) * 1024, 0, 16);
      *(LDS_AS u32x4*)(karea + (2 * t) * 1024 + lane * 16) = o0;
      *(LDS_AS u32x4*)(karea + (2 * t + 1) * 1024 + lane * 16) = o1;
    }
  }
  ST(18);
  f32x16 ac2[2][2];
  {
#pragma unroll
    for (int a = 0; a < 2; ++a)
#pragma unroll
      for (int c = 0; c < 2; ++c) ac2[a][c] = f32x16{};
    const unsigned woff = kh * 8192 + (8 * h + q4) * 256 + ((2 * wn) ^ q4) * 64 + g16 * 32 + u4 * 8;
    const unsigned ooff = kh * 8192 + (wr * 2) * 2048 + lane * 16;
    const lptr own0 = lds0 + (wr ? 5 : 9) * 16384 + kh * 8192 + lane * 16, own1 = lds0 + (wr ? 8 : 4) * 16384 + kh * 8192 + lane * 16;
    auto issue2 = [&](auto V, auto POS) {
      sfor<S2.n>([&](auto I) {
        constexpr Op o = S2.op[I];
        if constexpr (o.it == V && o.pos == POS) {
          if constexpr (o.tag >= TOU) ldOU(o.tag - TOU, o.piece); else ldWU(o.tag - TWU, o.piece);
        }
      });
    };
    sfor<16>([&](auto V) {
      constexpr int v = V;
      if constexpr (v == 0) {
        SB(); asm volatile("s_waitcnt vmcnt(0) lgkmcnt(0)" ::: "memory"); asm volatile("s_barrier" ::: "memory"); SB();
        if (tid == 0) __hip_atomic_store(sy1 + (1 + hp) * 16, tag1, RLX_AGENT);
      } else {
        if constexpr (v == POLL_V) {
          if (w == 0) {
            poll3(sy1 + 16, tag1, lane, hp);
            if constexpr (USE_ACQ) { __builtin_amdgcn_fence(__ATOMIC_ACQUIRE, "agent"); asm volatile("s_waitcnt vmcnt(0)" ::: "memory"); }
            if (hp == 0 && lane == 0) __hip_atomic_store(sy1, tag1, RLX_AGENT);
          }
        }
        SB(); wait_v<swait(S2, v, TWU + v, TOU + v)>(); asm volatile("s_barrier" ::: "memory"); SB();
      }
      ST(19 + v);
      sfor<2>([&](auto KS) {
        constexpr int ks = KS;
        bf16x8 wa[2], ob[2];
        const lptr wbase = lds0 + WSL[v] * 16384;
#pragma unroll
        for (int t = 0; t < 2; ++t) wa[t] = trfrag(wbase + (woff ^ (t << 6)), ks * 4096, ks * 4096 + 1024);
        if constexpr (v < 4) { ob[0] = ldsfrag(own0 + (2 * v + ks) * 1024); ob[1] = ldsfrag(own1 + (2 * v + ks) * 1024); }
        else {
#pragma unroll
          for (int t = 0; t < 2; ++t) ob[t] = ldsfrag(lds0 + OSL[v] * 16384 + ooff + t * 2048 + ks * 1024);
        }
        issue2(V, std::integral_constant<int, 2 * ks>{});
        ac2[0][0] = mfma(wa[0], ob[0], ac2[0][0]);
        ac2[0][1] = mfma(wa[0], ob[1], ac2[0][1]);
        issue2(V, std::integral_constant<int, 2 * ks + 1>{});
        ac2[1][0] = mfma(wa[1], ob[0], ac2[1][0]);
        ac2[1][1] = mfma(wa[1], ob[1], ac2[1][1]);
      });
      SB();
    });
  }
  ST(35);
  f32x16 fin[2];
  {
    const int es = (w >> 1) + ((w >> 1) == 3), eo = ((w ^ 4) >> 1) + (((w ^ 4) >> 1) == 3);
    const lptr my = lds0 + es * 16384 + (w & 1) * 8192 + lane * 16, other = lds0 + eo * 16384 + (w & 1) * 8192 + lane * 16;
#pragma unroll
    for (int c = 0; c < 2; ++c)
#pragma unroll
      for (int q = 0; q < 4; ++q) {
        f32x4 v;
#pragma unroll
        for (int e = 0; e < 4; ++e) v[e] = kh ? ac2[0][c][4 * q + e] : ac2[1][c][4 * q + e];
        *(LDS_AS f32x4*)(my + (c * 4 + q) * 1024) = v;
      }
    asm volatile("s_waitcnt lgkmcnt(0)" ::: "memory"); asm volatile("s_barrier" ::: "memory"); SB();
#pragma unroll
    for (int c = 0; c < 2; ++c)
#pragma unroll
      for (int q = 0; q < 4; ++q) {
        f32x4 v = *(LDS_AS f32x4*)(other + (c * 4 + q) * 1024);
#pragma unroll
        for (int e = 0; e < 4; ++e) fin[c][4 * q + e] = v[e] + (kh ? ac2[1][c][4 * q + e] : ac2[0][c][4 * q + e]);
      }
  }
  ST(36);
  {
    const __amdgpu_buffer_rsrc_t srsrc = __builtin_amdgcn_make_buffer_rsrc((void*)(ws + WS_SLAB), 0, 16 << 20, 0x00020000);
    const unsigned gbase = (unsigned)((br * 8 + rg) * 4 + hp) * 262144u + (wn * 2 + kh) * 4096 + lane * 16;
#pragma unroll
    for (int c = 0; c < 2; ++c) {
      const int bq = 2 * wr + c;
      if (bq != ho) {
#pragma unroll
        for (int q = 0; q < 4; ++q) {
          f32x4 v;
#pragma unroll
          for (int e = 0; e < 4; ++e) v[e] = fin[c][4 * q + e];
          __builtin_amdgcn_raw_buffer_store_b128(__builtin_bit_cast(u32x4, v), srsrc, gbase + bq * 65536 + ho * 16384 + q * 1024, 0, 16);
        }
      }
    }
    asm volatile("s_waitcnt vmcnt(0)" ::: "memory"); asm volatile("s_barrier" ::: "memory"); SB();
    if (tid == 0) __hip_atomic_store(sy3 + (1 + ho) * 16, tag3, RLX_AGENT);
    ST(37);
    if (wr == (ho >> 1)) {
      poll3(sy3 + 16, tag3, lane, ho);
      if constexpr (USE_ACQ) { __builtin_amdgcn_fence(__ATOMIC_ACQUIRE, "agent"); asm volatile("s_waitcnt vmcnt(0)" ::: "memory"); }
      if (ho == 0 && tid == 0) __hip_atomic_store(sy3, tag3, RLX_AGENT);
      ST(38);
      f32x16 s = (ho & 1) ? fin[1] : fin[0];
      u32x4 ld[3][4];
#pragma unroll
      for (int i = 0; i < 3; ++i) {
        const int src = (ho + 1 + i) & 3;
#pragma unroll
        for (int q = 0; q < 4; ++q) ld[i][q] = __builtin_amdgcn_raw_buffer_load_b128(srsrc, gbase + ho * 65536 + src * 16384 + q * 1024, 0, 16);
      }
#pragma unroll
      for (int i = 0; i < 3; ++i)
#pragma unroll
        for (int q = 0; q < 4; ++q) {
          const f32x4 v = __builtin_bit_cast(f32x4, ld[i][q]);
#pragma unroll
          for (int e = 0; e < 4; ++e) s[4 * q + e] += v[e];
        }
      u16* dst = out + (size_t)(rg * 128 + ho * 32 + r32) * 1024 + br * 512 + hp * 128 + wn * 64 + kh * 32 + 8 * h;
      *(u32x4*)(dst) = pack8(s, 0);
      *(u32x4*)(dst + 16) = pack8(s, 1);
    }
  }
  ST(39);
  ST_OUT(ws, 41);
}

extern "C" void kernel_launch(void* const* d_in, const int* in_sizes, int n_in, void* d_out, int out_size, void* d_ws, size_t ws_size, hipStream_t stream) {
  (void)hipFuncSetAttribute((const void*)fused, hipFuncAttributeMaxDynamicSharedMemorySize, LDS_BYTES);
  fused<<<256, 512, LDS_BYTES, stream>>>((const u16*)d_in[0], (const u16*)d_in[1], (const u16*)d_in[2], (const u16*)d_in[3], (const u16*)d_in[4],
                                         (const u16*)d_in[5], (const u16*)d_in[6], (const u16*)d_in[7], (const u16*)d_in[8], (const u16*)d_in[9],
                                         (char*)d_ws, (u16*)d_out);
}
```

```cpp
#include <hip/hip_runtime.h>
#include <hip/hip_bf16.h>
#include <utility>

typedef unsigned short u16;
typedef unsigned long long u64;
typedef __attribute__((ext_vector_type(4))) short s16x4;
typedef __attribute__((ext_vector_type(8))) short s16x8;
typedef __attribute__((ext_vector_type(8))) __bf16 bf16x8;
typedef __attribute__((ext_vector_type(2))) __bf16 bf16x2;
typedef __attribute__((ext_vector_type(2))) float f32x2;
typedef __attribute__((ext_vector_type(4))) float f32x4;
typedef __attribute__((ext_vector_type(16))) float f32x16;
typedef __attribute__((ext_vector_type(4))) unsigned u32x4;
#define LDS_AS __attribute__((address_space(3)))
#define GAS __attribute__((address_space(1)))
#define DI __device__ __forceinline__
typedef LDS_AS char* lptr;
typedef GAS u64 gu64;
#define RLX_AGENT __ATOMIC_RELAXED, __HIP_MEMORY_SCOPE_AGENT

#define CODE_PF 23040
#define POL_ALL 0
#define WARM_NT 1
#define USE_ACQ 0
#define POLL_V 2

template <int N> DI void wait_v() { asm volatile("s_waitcnt vmcnt(%0)" ::"n"(N) : "memory"); }
#define SB() __builtin_amdgcn_sched_barrier(0)

DI bf16x8 as_bf(s16x8 v) { return __builtin_bit_cast(bf16x8, v); }
DI bf16x8 as_bf(u32x4 v) { return __builtin_bit_cast(bf16x8, v); }
DI unsigned pk2(float a, float b) { bf16x2 r = __builtin_convertvector(f32x2{a, b}, bf16x2); return __builtin_bit_cast(unsigned, r); }
DI float bfr(float a) { bf16x2 r = __builtin_convertvector(f32x2{a, a}, bf16x2); return __uint_as_float(__builtin_bit_cast(unsigned, r) << 16); }
DI f32x16 mfma(bf16x8 a, bf16x8 b, f32x16 c) { return __builtin_amdgcn_mfma_f32_32x32x16_bf16(a, b, c, 0, 0, 0); }
DI s16x4 trread(lptr addr) { return __builtin_amdgcn_ds_read_tr16_b64_v4i16((LDS_AS s16x4*)addr); }
DI bf16x8 trfrag(lptr addr, int off0, int off1) {
  s16x4 lo = trread(addr + off0), hi = trread(addr + off1);
  return as_bf(__builtin_shufflevector(lo, hi, 0, 1, 2, 3, 4, 5, 6, 7));
}
DI bf16x8 ldsfrag(lptr addr) { return *(LDS_AS bf16x8*)addr; }
template <int POL> DI void glds16(unsigned voff, const void* sbase, lptr lds_addr) {
  unsigned keep; const unsigned dst = (unsigned)(size_t)lds_addr;
  if constexpr (POL == 1)
    asm volatile("s_mov_b32 %0, m0\n\ts_mov_b32 m0, %2\n\ts_nop 0\n\tglobal_load_lds_dwordx4 %1, %3 nt\n\ts_mov_b32 m0, %0"
                 : "=&s"(keep) : "v"(voff), "s"(dst), "s"(sbase) : "memory");
  else if constexpr (POL == 2)
    asm volatile("s_mov_b32 %0, m0\n\ts_mov_b32 m0, %2\n\ts_nop 0\n\tglobal_load_lds_dwordx4 %1, %3 sc1\n\ts_mov_b32 m0, %0"
                 : "=&s"(keep) : "v"(voff), "s"(dst), "s"(sbase) : "memory");
  else
    asm volatile("s_mov_b32 %0, m0\n\ts_mov_b32 m0, %2\n\ts_nop 0\n\tglobal_load_lds_dwordx4 %1, %3\n\ts_mov_b32 m0, %0"
                 : "=&s"(keep) : "v"(voff), "s"(dst), "s"(sbase) : "memory");
}
DI u32x4 ldpol(const u32x4* p) { if constexpr (POL_ALL) return __builtin_nontemporal_load(p); else return *p; }
DI u32x4 pack8(const f32x16& a, int s) {
  u32x4 r;
  r[0] = pk2(a[8 * s + 0], a[8 * s + 1]); r[1] = pk2(a[8 * s + 2], a[8 * s + 3]);
  r[2] = pk2(a[8 * s + 4], a[8 * s + 5]); r[3] = pk2(a[8 * s + 6], a[8 * s + 7]);
  return r;
}
template <int... Is, class F> DI void sfor_impl(std::integer_sequence<int, Is...>, F&& f) { (f(std::integral_constant<int, Is>{}), ...); }
template <int N, class F> DI void sfor(F&& f) { sfor_impl(std::make_integer_sequence<int, N>{}, f); }

#define ST_DECL(n)
#define ST(i)
#define ST_OUT(ws, n)

struct Op { int it, pos, tag, piece; };
enum { TX = 100, TW = 200, TKV = 300, TWARM = 400, TWU = 500, TOU = 600 };
struct Sched { Op op[128]; int n; };
#define KV_PRO 16
#define WOW_U 6
constexpr Sched make1() {
  Sched s{}; int n = 0;
  const int pro[6] = {TX + 0, TW + 0, TW + 1, TX + 1, TW + 2, TX + 2};
  for (int i = 0; i < 6; ++i) { s.op[n++] = Op{-1, i / 2, pro[i], 0}; s.op[n++] = Op{-1, i / 2, pro[i], 1}; }
  int kv = 0;
  for (; kv < KV_PRO; ++kv) s.op[n++] = Op{-1, 3, TKV, kv};
  for (int u = 0; u < 16; ++u) {
    const bool hw = u >= 1 && u + 2 < 16, hx = u >= 2 && !(u & 1) && u / 2 + 2 < 8;
    if (hw) s.op[n++] = Op{u, 0, TW + u + 2, 0};
    if (hx) s.op[n++] = Op{u, 1, TX + u / 2 + 2, 0};
    if (kv < 16) s.op[n++] = Op{u, 1, TKV, kv++};
    if (hw) s.op[n++] = Op{u, 2, TW + u + 2, 1};
    if (hx) s.op[n++] = Op{u, 3, TX + u / 2 + 2, 1};
    if (kv < 16) s.op[n++] = Op{u, 3, TKV, kv++};
    if (u == WOW_U) s.op[n++] = Op{u, 3, TWARM, 0};
    if (u == 12) { s.op[n++] = Op{u, 3, TWU + 0, 0}; s.op[n++] = Op{u, 3, TWU + 0, 1}; }
    if (u == 14) { s.op[n++] = Op{u, 0, TWU + 1, 0}; s.op[n++] = Op{u, 1, TWU + 1, 1}; s.op[n++] = Op{u, 2, TWU + 2, 0}; s.op[n++] = Op{u, 3, TWU + 2, 1}; }
    if (u == 15) { s.op[n++] = Op{u, 0, TWU + 3, 0}; s.op[n++] = Op{u, 2, TWU + 3, 1}; }
  }
  s.n = n; return s;
}
constexpr Sched S1 = make1();
constexpr int swait(const Sched& s, int it, int tagA, int tagB) {
  int issued = 0, last = -1;
  for (int i = 0; i < s.n; ++i) if (s.op[i].it < it) { if (s.op[i].tag == tagA || s.op[i].tag == tagB) last = i; issued = i + 1; }
  if (last < 0) return 63;
  const int r = issued - 1 - last; return r > 63 ? 63 : r;
}
constexpr int WSL[16] = {5, 1, 3, 2, 0, 1, 3, 6, 8, 0, 1, 3, 6, 8, 0, 1};
constexpr int OSL[16] = {-1, -1, -1, -1, 4, 5, 2, 7, 9, 4, 5, 2, 7, 9, 4, 5};
constexpr Sched make2() {
  Sched s{}; int n = 0;
  auto add = [&](int v, int pos, int tag) { s.op[n++] = Op{v, pos, tag, 0}; s.op[n++] = Op{v, pos + 1 > 3 ? 3 : pos + 1, tag, 1}; };
  add(2, 0, TOU + 4); add(2, 1, TOU + 5); add(2, 2, TWU + 5);
  add(3, 0, TWU + 6);
  add(4, 0, TOU + 6); add(4, 1, TWU + 7); add(4, 2, TOU + 7); add(4, 3, TWU + 8); add(4, 3, TOU + 8);
  for (int v = 5; v <= 11; ++v) { add(v, 0, TWU + v + 4); add(v, 2, TOU + v + 4); }
  for (int i = 1; i < n; ++i) { Op o = s.op[i]; int j = i - 1; while (j >= 0 && (s.op[j].it > o.it || (s.op[j].it == o.it && s.op[j].pos > o.pos))) { s.op[j + 1] = s.op[j]; --j; } s.op[j + 1] = o; }
  s.n = n; return s;
}
constexpr Sched S2 = make2();

constexpr int LDS_BYTES = 163840;
constexpr size_t WS_SLAB = 16u << 20, WS_SYNC = 32u << 20;

DI bool poll3(gu64* f0, u64 tag, int lane, int skip) {
  for (unsigned spins = 0; spins < (1u << 22); ++spins) {
    u64 v = tag;
    if (lane < 4 && lane != skip) v = __hip_atomic_load(f0 + lane * 16, RLX_AGENT);
    if (__all(v == tag)) return true;
    __builtin_amdgcn_s_sleep(1);
  }
  return false;
}

__global__ __launch_bounds__(512, 2) void fused(const u16* __restrict__ xA, const u16* __restrict__ kA, const u16* __restrict__ vA, const u16* __restrict__ wqA, const u16* __restrict__ woA,
                                                const u16* __restrict__ xB, const u16* __restrict__ kB, const u16* __restrict__ vB, const u16* __restrict__ wqB, const u16* __restrict__ woB,
                                                char* __restrict__ ws, u16* __restrict__ out) {
  extern __shared__ __attribute__((aligned(16))) char smem[];
  const lptr lds0 = (lptr)smem;
  const int id = blockIdx.x, g = id & 7, j = id >> 3;
  const int br = g >> 2, ho = g & 3, rg = j >> 2, hp = j & 3;
  const int tid = threadIdx.x, lane = tid & 63;
  const int w = __builtin_amdgcn_readfirstlane(tid >> 6);
  const u16* x = br ? xB : xA; const u16* kc = br ? kB : kA; const u16* vc = br ? vB : vA; const u16* wq = br ? wqB : wqA; const u16* wo = br ? woB : woA;
  ST_DECL(44); ST(0);
  unsigned cpf = 0;
  {
    const char* pc = (const char*)__builtin_amdgcn_s_getpc();
    const unsigned line = (unsigned)(j * 8 + w) * 64 + lane;
    if (w == 7 - (j & 7) && (unsigned)((j & 3) * 64 + lane) * 128 < CODE_PF)
      asm volatile("global_load_dword %0, %1, %2" : "=v"(cpf) : "v"((unsigned)((j & 3) * 64 + lane) * 128), "s"(pc) : "memory");
    (void)line;
  }
  gu64* sy1 = (gu64*)(ws + WS_SYNC) + (size_t)((br * 4 + ho) * 8 + rg) * 128;
  gu64* sy3 = (gu64*)(ws + WS_SYNC) + (size_t)(64 + (br * 8 + rg) * 4 + hp) * 128;
  u64 ep1, ep3;
  asm volatile("s_load_dwordx2 %0, %2, 0x0\n\ts_load_dwordx2 %1, %3, 0x0" : "=&s"(ep1), "=&s"(ep3) : "s"(sy1), "s"(sy3) : "memory");

  const int r32 = lane & 31, h = lane >> 5;
  const int q4 = (lane & 15) >> 2, p4 = lane & 3, g16 = (lane >> 4) & 1, u4 = ((p4 & 1) << 1) | (p4 >> 1);
  const int head0 = ho * 8 + hp * 2;
  f32x16 acc[4];
  const int kh = w >> 2, wn = (w >> 1) & 1, wr = w & 1, w4 = w & 3;
  unsigned wusrc[2];
#pragma unroll
  for (int i = 0; i < 2; ++i) {
    const int kr = (2 * w4 + i) * 4 + (lane >> 4), c = (lane & 15) ^ ((kr & 3) << 2);
    wusrc[i] = ((kh * 128 + kr) * 512 + 8 * c) * 2;
  }
  const u16* wub = wo + (size_t)(ho * 1024) * 512 + hp * 128;
  const unsigned ousrc = (((w4 * 32 + kh) * 8 * 64 + lane) * 8) * 2;
  const u16* oub = (const u16*)ws + (size_t)((br * 32 + rg * 4) * 32 + ho * 8) * 4096;
  auto ldWU = [&](int v, int i) {
    const int hq = (hp + (v >> 2)) & 3;
    glds16<0>(wusrc[i], wub + (size_t)(hq * 256 + (v & 3) * 32) * 512, lds0 + WSL[v] * 16384 + kh * 8192 + (2 * w4 + i) * 1024);
  };
  auto ldOU = [&](int v, int i) {
    const int hq = (hp + (v >> 2)) & 3;
    glds16<2>(ousrc, oub + (size_t)(hq * 2) * 4096 + ((v & 3) * 2 + i) * 512, lds0 + OSL[v] * 16384 + kh * 8192 + (2 * w4 + i) * 1024);
  };
  {
    const int bi = w >> 1, hi = w & 1;
    const int b = rg * 4 + bi, head = head0 + hi;
    unsigned xsrc[2], wsrc[2];
#pragma unroll
    for (int i = 0; i < 2; ++i) {
      const int row = (2 * w + i) * 8 + (lane >> 3), c = (lane & 7) ^ ((row >> 1) & 7);
      xsrc[i] = (row * 512 + 8 * c) * 2;
      const int kr = (2 * w + i) * 2 + (lane >> 5), c2 = (lane & 31) ^ ((kr & 3) << 2);
      wsrc[i] = (kr * 4096 + 8 * c2) * 2;
    }
    const u16* xb0 = x + (size_t)rg * 128 * 512;
    const u16* wb0 = wq + head0 * 128;
    const unsigned wowsrc = ((w * 16 + (lane >> 2)) * 512 + (lane & 3) * 32) * 2;
    unsigned wowarm = 0;
    const u16* kbase = kc + (size_t)(b * 32 + head) * 4096 + r32 * 128 + 8 * h;
    const u16* vbase = vc + (size_t)(b * 32 + head) * 4096;
    const unsigned vsrc = ((lane >> 4) * 128 + 8 * ((lane & 15) ^ ((lane >> 4) << 2))) * 2;
    const lptr varea = lds0 + 6 * 16384 + w * 8192;
    bf16x8 kf[8];
    auto issue = [&](auto IT, auto POS) {
      sfor<S1.n>([&](auto I) {
        constexpr Op o = S1.op[I];
        if constexpr (o.it == IT && o.pos == POS) {
          if constexpr (o.tag >= TWU) ldWU(o.tag - TWU, o.piece);
          else if constexpr (o.tag >= TWARM) asm volatile("global_load_dword %0, %1, %2" : "=v"(wowarm) : "v"(wowsrc), "s"(wub + (size_t)(rg * 128) * 512) : "memory");
          else if constexpr (o.tag >= TKV) {
            if constexpr (o.piece < 8) glds16<0>(vsrc, vbase + o.piece * 512, varea + o.piece * 1024);
            else kf[o.piece - 8] = *(const bf16x8*)(kbase + 16 * (o.piece - 8));
          } else if constexpr (o.tag >= TW) glds16<0>(wsrc[o.piece], wb0 + (size_t)32 * (o.tag - TW) * 4096, lds0 + ((o.tag - TW) % 3) * 16384 + (2 * w + o.piece) * 1024);
          else glds16<0>(xsrc[o.piece], xb0 + 64 * (o.tag - TX), lds0 + (3 + (o.tag - TX) % 3) * 16384 + (2 * w + o.piece) * 1024);
        }
      });
    };
    sfor<4>([&](auto G) { issue(std::integral_constant<int, -1>{}, G); if constexpr (G < 3) asm volatile("s_barrier" ::: "memory"); });

    const int xrow = bi * 32 + r32;
    const unsigned xoff = 3 * 16384 + xrow * 128 + ((h ^ ((xrow >> 1) & 7)) << 4);
    const unsigned woff = (8 * h + q4) * 512 + (4 * hi + q4) * 64 + g16 * 32 + u4 * 8;
#pragma unroll
    for (int t = 0; t < 4; ++t) acc[t] = f32x16{};
    bf16x8 xb[2], wa[2][4];
    auto rdfrags = [&](int kk) {
      const int u = kk >> 1, ks = kk & 1, X = kk >> 2, ks4 = kk & 3, buf = kk & 1;
      xb[buf] = ldsfrag(lds0 + (X % 3) * 16384 + (xoff ^ (ks4 << 5)));
#pragma unroll
      for (int t = 0; t < 4; ++t) wa[buf][t] = trfrag(lds0 + (u % 3) * 16384 + (woff ^ (t << 6)), ks * 8192, ks * 8192 + 2048);
    };
    sfor<16>([&](auto U) {
      constexpr int u = U;
      SB(); wait_v<swait(S1, u, TW + u, TX + u / 2)>(); asm volatile("s_barrier" ::: "memory"); SB();
      ST(1 + u);
      rdfrags(2 * u); rdfrags(2 * u + 1);
      if constexpr (u == 11) {
        asm volatile("" :: "v"(kf[0]), "v"(kf[1]), "v"(kf[2]), "v"(kf[3]), "v"(kf[4]), "v"(kf[5]), "v"(kf[6]), "v"(kf[7]));
      }
      issue(U, std::integral_constant<int, 0>{});
      acc[0] = mfma(wa[0][0], xb[0], acc[0]); acc[1] = mfma(wa[0][1], xb[0], acc[1]);
      issue(U, std::integral_constant<int, 1>{});
      acc[2] = mfma(wa[0][2], xb[0], acc[2]); acc[3] = mfma(wa[0][3], xb[0], acc[3]);
      issue(U, std::integral_constant<int, 2>{});
      acc[0] = mfma(wa[1][0], xb[1], acc[0]); acc[1] = mfma(wa[1][1], xb[1], acc[1]);
      issue(U, std::integral_constant<int, 3>{});
      acc[2] = mfma(wa[1][2], xb[1], acc[2]); acc[3] = mfma(wa[1][3], xb[1], acc[3]);
    });
    asm volatile("" :: "v"(cpf));
    asm volatile("" :: "v"(wowarm));
    SB(); asm volatile("s_waitcnt lgkmcnt(0)\n\ts_barrier" : "+s"(ep1), "+s"(ep3) :: "memory"); SB();
    ST(17);
    ldWU(4, 0); ldWU(4, 1);
    f32x16 sc = f32x16{};
#pragma unroll
    for (int t = 0; t < 4; ++t)
#pragma unroll
      for (int s2 = 0; s2 < 2; ++s2) sc = mfma(kf[2 * t + s2], as_bf(pack8(acc[t], s2)), sc);
    constexpr float SCALE = 0.08838834764831845f, LOG2E = 1.4426950408889634f;
    float m = -3.0e38f;
#pragma unroll
    for (int i = 0; i < 16; ++i) { sc[i] = bfr(bfr(sc[i]) * SCALE); m = fmaxf(m, sc[i]); }
    m = fmaxf(m, __shfl_xor(m, 32));
    float sum = 0.f;
#pragma unroll
    for (int i = 0; i < 16; ++i) { sc[i] = __builtin_amdgcn_exp2f((sc[i] - m) * LOG2E); sum += sc[i]; }
    sum += __shfl_xor(sum, 32);
    const float inv = 1.0f / sum;
#pragma unroll
    for (int i = 0; i < 16; ++i) sc[i] *= inv;
    const bf16x8 pf0 = as_bf(pack8(sc, 0)), pf1 = as_bf(pack8(sc, 1));
    const unsigned voff = (4 * h + q4) * 256 + q4 * 64 + g16 * 32 + u4 * 8;
    const __amdgpu_buffer_rsrc_t orsrc = __builtin_amdgcn_make_buffer_rsrc((void*)ws, 0, 16 << 20, 0x00020000);
    const unsigned ooff = (((br * 32 + b) * 32 + head) * 8 * 64 + lane) * 16;
#pragma unroll
    for (int t = 0; t < 4; ++t) {
      acc[t] = f32x16{};
      acc[t] = mfma(trfrag(varea + (voff ^ (t << 6)), 0, 2048), pf0, acc[t]);
      acc[t] = mfma(trfrag(varea + (voff ^ (t << 6)), 4096, 4096 + 2048), pf1, acc[t]);
    }
#pragma unroll
    for (int t = 0; t < 4; ++t) {
      const u32x4 o0 = pack8(acc[t], 0), o1 = pack8(acc[t], 1);
      __builtin_amdgcn_raw_buffer_store_b128(o0, orsrc, ooff + (2 * t) * 1024, 0, 16);
      __builtin_amdgcn_raw_buffer_store_b128(o1, orsrc, ooff + (2 * t + 1) * 1024, 0, 16);
      *(LDS_AS u32x4*)(varea + (2 * t) * 1024 + lane * 16) = o0;
      *(LDS_AS u32x4*)(varea + (2 * t + 1) * 1024 + lane * 16) = o1;
    }
  }
  ST(18);
  f32x16 ac2[2][2];
  {
#pragma unroll
    for (int a = 0; a < 2; ++a)
#pragma unroll
      for (int c = 0; c < 2; ++c) ac2[a][c] = f32x16{};
    const unsigned woff = kh * 8192 + (8 * h + q4) * 256 + ((2 * wn) ^ q4) * 64 + g16 * 32 + u4 * 8;
    const unsigned ooff = kh * 8192 + (wr * 2) * 2048 + lane * 16;
    const lptr own0 = lds0 + (6 + 2 * wr) * 16384 + kh * 8192 + lane * 16, own1 = own0 + 16384;
    auto issue2 = [&](auto V, auto POS) {
      sfor<S2.n>([&](auto I) {
        constexpr Op o = S2.op[I];
        if constexpr (o.it == V && o.pos == POS) {
          if constexpr (o.tag >= TOU) ldOU(o.tag - TOU, o.piece); else ldWU(o.tag - TWU, o.piece);
        }
      });
    };
    sfor<16>([&](auto V) {
      constexpr int v = V;
      if constexpr (v == 0) {
        SB(); asm volatile("s_waitcnt vmcnt(0) lgkmcnt(0)" ::: "memory"); asm volatile("s_barrier" ::: "memory"); SB();
        if (tid == 0) __hip_atomic_store(sy1 + (1 + hp) * 16, (ep1 + 1), RLX_AGENT);
      } else {
        if constexpr (v == POLL_V) {
          if (w == 0) {
            poll3(sy1 + 16, (ep1 + 1), lane, hp);
            if constexpr (USE_ACQ) { __builtin_amdgcn_fence(__ATOMIC_ACQUIRE, "agent"); asm volatile("s_waitcnt vmcnt(0)" ::: "memory"); }
            if (hp == 0 && lane == 0) __hip_atomic_store(sy1, (ep1 + 1), RLX_AGENT);
          }
        }
        SB(); wait_v<swait(S2, v, TWU + v, TOU + v)>(); asm volatile("s_barrier" ::: "memory"); SB();
      }
      ST(19 + v);
      sfor<2>([&](auto KS) {
        constexpr int ks = KS;
        bf16x8 wa[2], ob[2];
        const lptr wbase = lds0 + WSL[v] * 16384;
#pragma unroll
        for (int t = 0; t < 2; ++t) wa[t] = trfrag(wbase + (woff ^ (t << 6)), ks * 4096, ks * 4096 + 1024);
        if constexpr (v < 4) { ob[0] = ldsfrag(own0 + (2 * v + ks) * 1024); ob[1] = ldsfrag(own1 + (2 * v + ks) * 1024); }
        else {
#pragma unroll
          for (int t = 0; t < 2; ++t) ob[t] = ldsfrag(lds0 + OSL[v] * 16384 + ooff + t * 2048 + ks * 1024);
        }
        issue2(V, std::integral_constant<int, 2 * ks>{});
        ac2[0][0] = mfma(wa[0], ob[0], ac2[0][0]);
        ac2[0][1] = mfma(wa[0], ob[1], ac2[0][1]);
        issue2(V, std::integral_constant<int, 2 * ks + 1>{});
        ac2[1][0] = mfma(wa[1], ob[0], ac2[1][0]);
        ac2[1][1] = mfma(wa[1], ob[1], ac2[1][1]);
      });
      SB();
    });
  }
  ST(35);
  f32x16 fin[2];
  {
    const lptr my = lds0 + 6 * 16384 + w * 8192 + lane * 16, other = lds0 + 6 * 16384 + (w ^ 4) * 8192 + lane * 16;
#pragma unroll
    for (int c = 0; c < 2; ++c)
#pragma unroll
      for (int q = 0; q < 4; ++q) {
        f32x4 v;
#pragma unroll
        for (int e = 0; e < 4; ++e) v[e] = kh ? ac2[0][c][4 * q + e] : ac2[1][c][4 * q + e];
        *(LDS_AS f32x4*)(my + (c * 4 + q) * 1024) = v;
      }
    asm volatile("s_waitcnt lgkmcnt(0)" ::: "memory"); asm volatile("s_barrier" ::: "memory"); SB();
#pragma unroll
    for (int c = 0; c < 2; ++c)
#pragma unroll
      for (int q = 0; q < 4; ++q) {
        f32x4 v = *(LDS_AS f32x4*)(other + (c * 4 + q) * 1024);
#pragma unroll
        for (int e = 0; e < 4; ++e) fin[c][4 * q + e] = v[e] + (kh ? ac2[1][c][4 * q + e] : ac2[0][c][4 * q + e]);
      }
  }
  ST(36);
  {
    const __amdgpu_buffer_rsrc_t srsrc = __builtin_amdgcn_make_buffer_rsrc((void*)(ws + WS_SLAB), 0, 16 << 20, 0x00020000);
    const unsigned gbase = (unsigned)((br * 8 + rg) * 4 + hp) * 262144u + (wn * 2 + kh) * 4096 + lane * 16;
#pragma unroll
    for (int c = 0; c < 2; ++c) {
      const int bq = 2 * wr + c;
      if (bq != ho) {
#pragma unroll
        for (int q = 0; q < 4; ++q) {
          f32x4 v;
#pragma unroll
          for (int e = 0; e < 4; ++e) v[e] = fin[c][4 * q + e];
          __builtin_amdgcn_raw_buffer_store_b128(__builtin_bit_cast(u32x4, v), srsrc, gbase + bq * 65536 + ho * 16384 + q * 1024, 0, 16);
        }
      }
    }
    asm volatile("s_waitcnt vmcnt(0)" ::: "memory"); asm volatile("s_barrier" ::: "memory"); SB();
    if (tid == 0) __hip_atomic_store(sy3 + (1 + ho) * 16, (ep3 + 1), RLX_AGENT);
    ST(37);
    if (wr == (ho >> 1)) {
      poll3(sy3 + 16, (ep3 + 1), lane, ho);
      if constexpr (USE_ACQ) { __builtin_amdgcn_fence(__ATOMIC_ACQUIRE, "agent"); asm volatile("s_waitcnt vmcnt(0)" ::: "memory"); }
      if (ho == 0 && tid == 0) __hip_atomic_store(sy3, (ep3 + 1), RLX_AGENT);
      ST(38);
      f32x16 s = (ho & 1) ? fin[1] : fin[0];
      u32x4 ld[3][4];
#pragma unroll
      for (int i = 0; i < 3; ++i) {
        const int src = (ho + 1 + i) & 3;
#pragma unroll
        for (int q = 0; q < 4; ++q) ld[i][q] = __builtin_amdgcn_raw_buffer_load_b128(srsrc, gbase + ho * 65536 + src * 16384 + q * 1024, 0, 16);
      }
#pragma unroll
      for (int i = 0; i < 3; ++i)
#pragma unroll
        for (int q = 0; q < 4; ++q) {
          const f32x4 v = __builtin_bit_cast(f32x4, ld[i][q]);
#pragma unroll
          for (int e = 0; e < 4; ++e) s[4 * q + e] += v[e];
        }
      u16* dst = out + (size_t)(rg * 128 + ho * 32 + r32) * 1024 + br * 512 + hp * 128 + wn * 64 + kh * 32 + 8 * h;
      *(u32x4*)(dst) = pack8(s, 0);
      *(u32x4*)(dst + 16) = pack8(s, 1);
    }
  }
  ST(39);
  ST_OUT(ws, 41);
}

extern "C" void kernel_launch(void* const* d_in, const int* in_sizes, int n_in, void* d_out, int out_size, void* d_ws, size_t ws_size, hipStream_t stream) {
  (void)hipFuncSetAttribute((const void*)fused, hipFuncAttributeMaxDynamicSharedMemorySize, LDS_BYTES);
  fused<<<256, 512, LDS_BYTES, stream>>>((const u16*)d_in[0], (const u16*)d_in[1], (const u16*)d_in[2], (const u16*)d_in[3], (const u16*)d_in[4],
                                         (const u16*)d_in[5], (const u16*)d_in[6], (const u16*)d_in[7], (const u16*)d_in[8], (const u16*)d_in[9],
                                         (char*)d_ws, (u16*)d_out);
}
```

```cpp
#include <hip/hip_runtime.h>
#include <hip/hip_bf16.h>
#include <utility>

typedef unsigned short u16;
typedef unsigned long long u64;
typedef __attribute__((ext_vector_type(4))) short s16x4;
typedef __attribute__((ext_vector_type(8))) short s16x8;
typedef __attribute__((ext_vector_type(8))) __bf16 bf16x8;
typedef __attribute__((ext_vector_type(2))) __bf16 bf16x2;
typedef __attribute__((ext_vector_type(2))) float f32x2;
typedef __attribute__((ext_vector_type(4))) float f32x4;
typedef __attribute__((ext_vector_type(16))) float f32x16;
typedef __attribute__((ext_vector_type(4))) unsigned u32x4;
#define LDS_AS __attribute__((address_space(3)))
#define GAS __attribute__((address_space(1)))
#define DI __device__ __forceinline__
typedef LDS_AS char* lptr;
typedef GAS u64 gu64;
#define RLX_AGENT __ATOMIC_RELAXED, __HIP_MEMORY_SCOPE_AGENT

#define CODE_PF 23040
#define POL_ALL 0
#define WARM_NT 1
#define USE_ACQ 0
#define POLL_V 2

template <int N> DI void wait_v() { asm volatile("s_waitcnt vmcnt(%0)" ::"n"(N) : "memory"); }
#define SB() __builtin_amdgcn_sched_barrier(0)

DI bf16x8 as_bf(s16x8 v) { return __builtin_bit_cast(bf16x8, v); }
DI bf16x8 as_bf(u32x4 v) { return __builtin_bit_cast(bf16x8, v); }
DI unsigned pk2(float a, float b) { bf16x2 r = __builtin_convertvector(f32x2{a, b}, bf16x2); return __builtin_bit_cast(unsigned, r); }
DI float bfr(float a) { bf16x2 r = __builtin_convertvector(f32x2{a, a}, bf16x2); return __uint_as_float(__builtin_bit_cast(unsigned, r) << 16); }
DI f32x16 mfma(bf16x8 a, bf16x8 b, f32x16 c) { return __builtin_amdgcn_mfma_f32_32x32x16_bf16(a, b, c, 0, 0, 0); }
DI s16x4 trread(lptr addr) { return __builtin_amdgcn_ds_read_tr16_b64_v4i16((LDS_AS s16x4*)addr); }
DI bf16x8 trfrag(lptr addr, int off0, int off1) {
  s16x4 lo = trread(addr + off0), hi = trread(addr + off1);
  return as_bf(__builtin_shufflevector(lo, hi, 0, 1, 2, 3, 4, 5, 6, 7));
}
DI bf16x8 ldsfrag(lptr addr) { return *(LDS_AS bf16x8*)addr; }
template <int POL> DI void glds16(unsigned voff, const void* sbase, lptr lds_addr) {
  unsigned keep; const unsigned dst = (unsigned)(size_t)lds_addr;
  if constexpr (POL == 1)
    asm volatile("s_mov_b32 %0, m0\n\ts_mov_b32 m0, %2\n\ts_nop 0\n\tglobal_load_lds_dwordx4 %1, %3 nt\n\ts_mov_b32 m0, %0"
                 : "=&s"(keep) : "v"(voff), "s"(dst), "s"(sbase) : "memory");
  else if constexpr (POL == 2)
    asm volatile("s_mov_b32 %0, m0\n\ts_mov_b32 m0, %2\n\ts_nop 0\n\tglobal_load_lds_dwordx4 %1, %3 sc1\n\ts_mov_b32 m0, %0"
                 : "=&s"(keep) : "v"(voff), "s"(dst), "s"(sbase) : "memory");
  else
    asm volatile("s_mov_b32 %0, m0\n\ts_mov_b32 m0, %2\n\ts_nop 0\n\tglobal_load_lds_dwordx4 %1, %3\n\ts_mov_b32 m0, %0"
                 : "=&s"(keep) : "v"(voff), "s"(dst), "s"(sbase) : "memory");
}
DI u32x4 ldpol(const u32x4* p) { if constexpr (POL_ALL) return __builtin_nontemporal_load(p); else return *p; }
DI u32x4 pack8(const f32x16& a, int s) {
  u32x4 r;
  r[0] = pk2(a[8 * s + 0], a[8 * s + 1]); r[1] = pk2(a[8 * s + 2], a[8 * s + 3]);
  r[2] = pk2(a[8 * s + 4], a[8 * s + 5]); r[3] = pk2(a[8 * s + 6], a[8 * s + 7]);
  return r;
}
template <int... Is, class F> DI void sfor_impl(std::integer_sequence<int, Is...>, F&& f) { (f(std::integral_constant<int, Is>{}), ...); }
template <int N, class F> DI void sfor(F&& f) { sfor_impl(std::make_integer_sequence<int, N>{}, f); }

struct Op { int it, pos, tag, piece; };
enum { TX = 100, TW = 200, TKV = 300, TWARM = 400, TWU = 500, TOU = 600 };
struct Sched { Op op[128]; int n; };
#define KV_PRO 16
#define WOW_U 6
constexpr Sched make1() {
  Sched s{}; int n = 0;
  const int pro[6] = {TX + 0, TW + 0, TW + 1, TX + 1, TW + 2, TX + 2};
  for (int i = 0; i < 6; ++i) { s.op[n++] = Op{-1, i / 2, pro[i], 0}; s.op[n++] = Op{-1, i / 2, pro[i], 1}; }
  int kv = 0;
  for (; kv < KV_PRO; ++kv) s.op[n++] = Op{-1, 3, TKV, kv};
  for (int u = 0; u < 16; ++u) {
    const bool hw = u >= 1 && u + 2 < 16, hx = u >= 2 && !(u & 1) && u / 2 + 2 < 8;
    if (hw) s.op[n++] = Op{u, 0, TW + u + 2, 0};
    if (hx) s.op[n++] = Op{u, 1, TX + u / 2 + 2, 0};
    if (kv < 16) s.op[n++] = Op{u, 1, TKV, kv++};
    if (hw) s.op[n++] = Op{u, 2, TW + u + 2, 1};
    if (hx) s.op[n++] = Op{u, 3, TX + u / 2 + 2, 1};
    if (kv < 16) s.op[n++] = Op{u, 3, TKV, kv++};
    if (u == WOW_U) s.op[n++] = Op{u, 3, TWARM, 0};
    if (u == 12) { s.op[n++] = Op{u, 3, TWU + 0, 0}; s.op[n++] = Op{u, 3, TWU + 0, 1}; }
    if (u == 14) { s.op[n++] = Op{u, 0, TWU + 1, 0}; s.op[n++] = Op{u, 1, TWU + 1, 1}; s.op[n++] = Op{u, 2, TWU + 2, 0}; s.op[n++] = Op{u, 3, TWU + 2, 1}; }
    if (u == 15) { s.op[n++] = Op{u, 0, TWU + 3, 0}; s.op[n++] = Op{u, 2, TWU + 3, 1}; }
  }
  s.n = n; return s;
}
constexpr Sched S1 = make1();
constexpr int swait(const Sched& s, int it, int tagA, int tagB) {
  int issued = 0, last = -1;
  for (int i = 0; i < s.n; ++i) if (s.op[i].it < it) { if (s.op[i].tag == tagA || s.op[i].tag == tagB) last = i; issued = i + 1; }
  if (last < 0) return 63;
  const int r = issued - 1 - last; return r > 63 ? 63 : r;
}
constexpr int WSL[16] = {5, 1, 3, 2, 0, 1, 3, 6, 8, 0, 1, 3, 6, 8, 0, 1};
constexpr int OSL[16] = {-1, -1, -1, -1, 4, 5, 2, 7, 9, 4, 5, 2, 7, 9, 4, 5};
constexpr Sched make2() {
  Sched s{}; int n = 0;
  auto add = [&](int v, int pos, int tag) { s.op[n++] = Op{v, pos, tag, 0}; s.op[n++] = Op{v, pos + 1 > 3 ? 3 : pos + 1, tag, 1}; };
  add(2, 0, TOU + 4); add(2, 1, TOU + 5); add(2, 2, TWU + 5);
  add(3, 0, TWU + 6);
  add(4, 0, TOU + 6); add(4, 1, TWU + 7); add(4, 2, TOU + 7); add(4, 3, TWU + 8); add(4, 3, TOU + 8);
  for (int v = 5; v <= 11; ++v) { add(v, 0, TWU + v + 4); add(v, 2, TOU + v + 4); }
  for (int i = 1; i < n; ++i) { Op o = s.op[i]; int j = i - 1; while (j >= 0 && (s.op[j].it > o.it || (s.op[j].it == o.it && s.op[j].pos > o.pos))) { s.op[j + 1] = s.op[j]; --j; } s.op[j + 1] = o; }
  s.n = n; return s;
}
constexpr Sched S2 = make2();

constexpr int LDS_BYTES = 163840;
constexpr size_t WS_SLAB = 16u << 20, WS_SYNC = 32u << 20;

DI bool poll3(gu64* f0, u64 tag, int lane, int skip) {
  for (unsigned spins = 0; spins < (1u << 22); ++spins) {
    u64 v = tag;
    if (lane < 4 && lane != skip) v = __hip_atomic_load(f0 + lane * 16, RLX_AGENT);
    if (__all(v == tag)) return true;
    __builtin_amdgcn_s_sleep(1);
  }
  return false;
}

__global__ __launch_bounds__(512, 2) void fused(const u16* __restrict__ xA, const u16* __restrict__ kA, const u16* __restrict__ vA, const u16* __restrict__ wqA, const u16* __restrict__ woA,
                                                const u16* __restrict__ xB, const u16* __restrict__ kB, const u16* __restrict__ vB, const u16* __restrict__ wqB, const u16* __restrict__ woB,
                                                char* __restrict__ ws, u16* __restrict__ out) {
  extern __shared__ __attribute__((aligned(16))) char smem[];
  const lptr lds0 = (lptr)smem;
  const int id = blockIdx.x, g = id & 7, j = id >> 3;
  const int br = g >> 2, ho = g & 3, rg = j >> 2, hp = j & 3;
  const int tid = threadIdx.x, lane = tid & 63;
  const int w = __builtin_amdgcn_readfirstlane(tid >> 6);
  const u16* x = br ? xB : xA; const u16* kc = br ? kB : kA; const u16* vc = br ? vB : vA; const u16* wq = br ? wqB : wqA; const u16* wo = br ? woB : woA;
  unsigned cpf = 0;
  {
    const char* pc = (const char*)__builtin_amdgcn_s_getpc();
    const unsigned line = (unsigned)(j * 8 + w) * 64 + lane;
    if (w == 7 - (j & 7) && (unsigned)((j & 3) * 64 + lane) * 128 < CODE_PF)
      asm volatile("global_load_dword %0, %1, %2" : "=v"(cpf) : "v"((unsigned)((j & 3) * 64 + lane) * 128), "s"(pc) : "memory");
    (void)line;
  }
  gu64* sy1 = (gu64*)(ws + WS_SYNC) + (size_t)((br * 4 + ho) * 8 + rg) * 128;
  gu64* sy3 = (gu64*)(ws + WS_SYNC) + (size_t)(64 + (br * 8 + rg) * 4 + hp) * 128;
  u64 e1v, e3v;
  asm volatile("global_load_dwordx2 %0, %2, off sc1\n\tglobal_load_dwordx2 %1, %3, off sc1" : "=&v"(e1v), "=&v"(e3v) : "v"(sy1), "v"(sy3) : "memory");
  u64 ep1, ep3;

  const int r32 = lane & 31, h = lane >> 5;
  const int q4 = (lane & 15) >> 2, p4 = lane & 3, g16 = (lane >> 4) & 1, u4 = ((p4 & 1) << 1) | (p4 >> 1);
  const int head0 = ho * 8 + hp * 2;
  f32x16 acc[4];
  const int kh = w >> 2, wn = (w >> 1) & 1, wr = w & 1, w4 = w & 3;
  unsigned wusrc[2];
#pragma unroll
  for (int i = 0; i < 2; ++i) {
    const int kr = (2 * w4 + i) * 4 + (lane >> 4), c = (lane & 15) ^ ((kr & 3) << 2);
    wusrc[i] = ((kh * 128 + kr) * 512 + 8 * c) * 2;
  }
  const u16* wub = wo + (size_t)(ho * 1024) * 512 + hp * 128;
  const unsigned ousrc = (((w4 * 32 + kh) * 8 * 64 + lane) * 8) * 2;
  const u16* oub = (const u16*)ws + (size_t)((br * 32 + rg * 4) * 32 + ho * 8) * 4096;
  auto ldWU = [&](int v, int i) {
    const int hq = (hp + (v >> 2)) & 3;
    glds16<0>(wusrc[i], wub + (size_t)(hq * 256 + (v & 3) * 32) * 512, lds0 + WSL[v] * 16384 + kh * 8192 + (2 * w4 + i) * 1024);
  };
  auto ldOU = [&](int v, int i) {
    const int hq = (hp + (v >> 2)) & 3;
    glds16<2>(ousrc, oub + (size_t)(hq * 2) * 4096 + ((v & 3) * 2 + i) * 512, lds0 + OSL[v] * 16384 + kh * 8192 + (2 * w4 + i) * 1024);
  };
  {
    const int bi = w >> 1, hi = w & 1;
    const int b = rg * 4 + bi, head = head0 + hi;
    unsigned xsrc[2], wsrc[2];
#pragma unroll
    for (int i = 0; i < 2; ++i) {
      const int row = (2 * w + i) * 8 + (lane >> 3), c = (lane & 7) ^ ((row >> 1) & 7);
      xsrc[i] = (row * 512 + 8 * c) * 2;
      const int kr = (2 * w + i) * 2 + (lane >> 5), c2 = (lane & 31) ^ ((kr & 3) << 2);
      wsrc[i] = (kr * 4096 + 8 * c2) * 2;
    }
    const u16* xb0 = x + (size_t)rg * 128 * 512;
    const u16* wb0 = wq + head0 * 128;
    const unsigned wowsrc = ((w * 16 + (lane >> 2)) * 512 + (lane & 3) * 32) * 2;
    unsigned wowarm = 0;
    const u16* kbase = kc + (size_t)(b * 32 + head) * 4096 + r32 * 128 + 8 * h;
    const u16* vbase = vc + (size_t)(b * 32 + head) * 4096;
    const unsigned vsrc = ((lane >> 4) * 128 + 8 * ((lane & 15) ^ ((lane >> 4) << 2))) * 2;
    const lptr varea = lds0 + 6 * 16384 + w * 8192;
    bf16x8 kf[8];
    auto issue = [&](auto IT, auto POS) {
      sfor<S1.n>([&](auto I) {
        constexpr Op o = S1.op[I];
        if constexpr (o.it == IT && o.pos == POS) {
          if constexpr (o.tag >= TWU) ldWU(o.tag - TWU, o.piece);
          else if constexpr (o.tag >= TWARM) asm volatile("global_load_dword %0, %1, %2" : "=v"(wowarm) : "v"(wowsrc), "s"(wub + (size_t)(rg * 128) * 512) : "memory");
          else if constexpr (o.tag >= TKV) {
            if constexpr (o.piece < 8) glds16<0>(vsrc, vbase + o.piece * 512, varea + o.piece * 1024);
            else kf[o.piece - 8] = *(const bf16x8*)(kbase + 16 * (o.piece - 8));
          } else if constexpr (o.tag >= TW) glds16<0>(wsrc[o.piece], wb0 + (size_t)32 * (o.tag - TW) * 4096, lds0 + ((o.tag - TW) % 3) * 16384 + (2 * w + o.piece) * 1024);
          else glds16<0>(xsrc[o.piece], xb0 + 64 * (o.tag - TX), lds0 + (3 + (o.tag - TX) % 3) * 16384 + (2 * w + o.piece) * 1024);
        }
      });
    };
    sfor<4>([&](auto G) { issue(std::integral_constant<int, -1>{}, G); if constexpr (G < 3) asm volatile("s_barrier" ::: "memory"); });

    const int xrow = bi * 32 + r32;
    const unsigned xoff = 3 * 16384 + xrow * 128 + ((h ^ ((xrow >> 1) & 7)) << 4);
    const unsigned woff = (8 * h + q4) * 512 + (4 * hi + q4) * 64 + g16 * 32 + u4 * 8;
#pragma unroll
    for (int t = 0; t < 4; ++t) acc[t] = f32x16{};
    bf16x8 xb[2], wa[2][4];
    auto rdfrags = [&](int kk) {
      const int u = kk >> 1, ks = kk & 1, X = kk >> 2, ks4 = kk & 3, buf = kk & 1;
      xb[buf] = ldsfrag(lds0 + (X % 3) * 16384 + (xoff ^ (ks4 << 5)));
#pragma unroll
      for (int t = 0; t < 4; ++t) wa[buf][t] = trfrag(lds0 + (u % 3) * 16384 + (woff ^ (t << 6)), ks * 8192, ks * 8192 + 2048);
    };
    sfor<16>([&](auto U) {
      constexpr int u = U;
      SB(); wait_v<swait(S1, u, TW + u, TX + u / 2)>(); asm volatile("s_barrier" ::: "memory"); SB();
      rdfrags(2 * u); rdfrags(2 * u + 1);
      if constexpr (u == 11) {
        asm volatile("" :: "v"(kf[0]), "v"(kf[1]), "v"(kf[2]), "v"(kf[3]), "v"(kf[4]), "v"(kf[5]), "v"(kf[6]), "v"(kf[7]));
      }
      issue(U, std::integral_constant<int, 0>{});
      acc[0] = mfma(wa[0][0], xb[0], acc[0]); acc[1] = mfma(wa[0][1], xb[0], acc[1]);
      issue(U, std::integral_constant<int, 1>{});
      acc[2] = mfma(wa[0][2], xb[0], acc[2]); acc[3] = mfma(wa[0][3], xb[0], acc[3]);
      issue(U, std::integral_constant<int, 2>{});
      acc[0] = mfma(wa[1][0], xb[1], acc[0]); acc[1] = mfma(wa[1][1], xb[1], acc[1]);
      issue(U, std::integral_constant<int, 3>{});
      acc[2] = mfma(wa[1][2], xb[1], acc[2]); acc[3] = mfma(wa[1][3], xb[1], acc[3]);
    });
    asm volatile("" :: "v"(cpf));
    asm volatile("" :: "v"(wowarm));
    SB(); asm volatile("s_barrier" : "+v"(e1v), "+v"(e3v) :: "memory"); SB();
    ep1 = ((u64)(unsigned)__builtin_amdgcn_readfirstlane((int)(e1v >> 32)) << 32) | (unsigned)__builtin_amdgcn_readfirstlane((int)e1v);
    ep3 = ((u64)(unsigned)__builtin_amdgcn_readfirstlane((int)(e3v >> 32)) << 32) | (unsigned)__builtin_amdgcn_readfirstlane((int)e3v);
    ldWU(4, 0); ldWU(4, 1);
    f32x16 sc = f32x16{};
#pragma unroll
    for (int t = 0; t < 4; ++t)
#pragma unroll
      for (int s2 = 0; s2 < 2; ++s2) sc = mfma(kf[2 * t + s2], as_bf(pack8(acc[t], s2)), sc);
    constexpr float SCALE = 0.08838834764831845f, LOG2E = 1.4426950408889634f;
    float m = -3.0e38f;
#pragma unroll
    for (int i = 0; i < 16; ++i) { sc[i] = bfr(bfr(sc[i]) * SCALE); m = fmaxf(m, sc[i]); }
    m = fmaxf(m, __shfl_xor(m, 32));
    float sum = 0.f;
#pragma unroll
    for (int i = 0; i < 16; ++i) { sc[i] = __builtin_amdgcn_exp2f((sc[i] - m) * LOG2E); sum += sc[i]; }
    sum += __shfl_xor(sum, 32);
    const float inv = 1.0f / sum;
#pragma unroll
    for (int i = 0; i < 16; ++i) sc[i] *= inv;
    const bf16x8 pf0 = as_bf(pack8(sc, 0)), pf1 = as_bf(pack8(sc, 1));
    const unsigned voff = (4 * h + q4) * 256 + q4 * 64 + g16 * 32 + u4 * 8;
    const __amdgpu_buffer_rsrc_t orsrc = __builtin_amdgcn_make_buffer_rsrc((void*)ws, 0, 16 << 20, 0x00020000);
    const unsigned ooff = (((br * 32 + b) * 32 + head) * 8 * 64 + lane) * 16;
#pragma unroll
    for (int t = 0; t < 4; ++t) {
      acc[t] = f32x16{};
      acc[t] = mfma(trfrag(varea + (voff ^ (t << 6)), 0, 2048), pf0, acc[t]);
      acc[t] = mfma(trfrag(varea + (voff ^ (t << 6)), 4096, 4096 + 2048), pf1, acc[t]);
    }
#pragma unroll
    for (int t = 0; t < 4; ++t) {
      const u32x4 o0 = pack8(acc[t], 0), o1 = pack8(acc[t], 1);
      __builtin_amdgcn_raw_buffer_store_b128(o0, orsrc, ooff + (2 * t) * 1024, 0, 16);
      __builtin_amdgcn_raw_buffer_store_b128(o1, orsrc, ooff + (2 * t + 1) * 1024, 0, 16);
      *(LDS_AS u32x4*)(varea + (2 * t) * 1024 + lane * 16) = o0;
      *(LDS_AS u32x4*)(varea + (2 * t + 1) * 1024 + lane * 16) = o1;
    }
  }
  f32x16 ac2[2][2];
  {
#pragma unroll
    for (int a = 0; a < 2; ++a)
#pragma unroll
      for (int c = 0; c < 2; ++c) ac2[a][c] = f32x16{};
    const unsigned woff = kh * 8192 + (8 * h + q4) * 256 + ((2 * wn) ^ q4) * 64 + g16 * 32 + u4 * 8;
    const unsigned ooff = kh * 8192 + (wr * 2) * 2048 + lane * 16;
    const lptr own0 = lds0 + (6 + 2 * wr) * 16384 + kh * 8192 + lane * 16, own1 = own0 + 16384;
    auto issue2 = [&](auto V, auto POS) {
      sfor<S2.n>([&](auto I) {
        constexpr Op o = S2.op[I];
        if constexpr (o.it == V && o.pos == POS) {
          if constexpr (o.tag >= TOU) ldOU(o.tag - TOU, o.piece); else ldWU(o.tag - TWU, o.piece);
        }
      });
    };
    sfor<16>([&](auto V) {
      constexpr int v = V;
      if constexpr (v == 0) {
        SB(); asm volatile("s_waitcnt vmcnt(0) lgkmcnt(0)" ::: "memory"); asm volatile("s_barrier" ::: "memory"); SB();
        if (tid == 0) __hip_atomic_store(sy1 + (1 + hp) * 16, (ep1 + 1), RLX_AGENT);
      } else {
        if constexpr (v == POLL_V) {
          if (w == 0) {
            poll3(sy1 + 16, (ep1 + 1), lane, hp);
            if constexpr (USE_ACQ) { __builtin_amdgcn_fence(__ATOMIC_ACQUIRE, "agent"); asm volatile("s_waitcnt vmcnt(0)" ::: "memory"); }
            if (hp == 0 && lane == 0) __hip_atomic_store(sy1, (ep1 + 1), RLX_AGENT);
          }
        }
        SB(); wait_v<swait(S2, v, TWU + v, TOU + v)>(); asm volatile("s_barrier" ::: "memory"); SB();
      }
      sfor<2>([&](auto KS) {
        constexpr int ks = KS;
        bf16x8 wa[2], ob[2];
        const lptr wbase = lds0 + WSL[v] * 16384;
#pragma unroll
        for (int t = 0; t < 2; ++t) wa[t] = trfrag(wbase + (woff ^ (t << 6)), ks * 4096, ks * 4096 + 1024);
        if constexpr (v < 4) { ob[0] = ldsfrag(own0 + (2 * v + ks) * 1024); ob[1] = ldsfrag(own1 + (2 * v + ks) * 1024); }
        else {
#pragma unroll
          for (int t = 0; t < 2; ++t) ob[t] = ldsfrag(lds0 + OSL[v] * 16384 + ooff + t * 2048 + ks * 1024);
        }
        issue2(V, std::integral_constant<int, 2 * ks>{});
        ac2[0][0] = mfma(wa[0], ob[0], ac2[0][0]);
        ac2[0][1] = mfma(wa[0], ob[1], ac2[0][1]);
        issue2(V, std::integral_constant<int, 2 * ks + 1>{});
        ac2[1][0] = mfma(wa[1], ob[0], ac2[1][0]);
        ac2[1][1] = mfma(wa[1], ob[1], ac2[1][1]);
      });
      SB();
    });
  }
  f32x16 fin[2];
  {
    const lptr my = lds0 + 6 * 16384 + w * 8192 + lane * 16, other = lds0 + 6 * 16384 + (w ^ 4) * 8192 + lane * 16;
#pragma unroll
    for (int c = 0; c < 2; ++c)
#pragma unroll
      for (int q = 0; q < 4; ++q) {
        f32x4 v;
#pragma unroll
        for (int e = 0; e < 4; ++e) v[e] = kh ? ac2[0][c][4 * q + e] : ac2[1][c][4 * q + e];
        *(LDS_AS f32x4*)(my + (c * 4 + q) * 1024) = v;
      }
    asm volatile("s_waitcnt lgkmcnt(0)" ::: "memory"); asm volatile("s_barrier" ::: "memory"); SB();
#pragma unroll
    for (int c = 0; c < 2; ++c)
#pragma unroll
      for (int q = 0; q < 4; ++q) {
        f32x4 v = *(LDS_AS f32x4*)(other + (c * 4 + q) * 1024);
#pragma unroll
        for (int e = 0; e < 4; ++e) fin[c][4 * q + e] = v[e] + (kh ? ac2[1][c][4 * q + e] : ac2[0][c][4 * q + e]);
      }
  }
  {
    const __amdgpu_buffer_rsrc_t srsrc = __builtin_amdgcn_make_buffer_rsrc((void*)(ws + WS_SLAB), 0, 16 << 20, 0x00020000);
    const unsigned gbase = (unsigned)((br * 8 + rg) * 4 + hp) * 262144u + (wn * 2 + kh) * 4096 + lane * 16;
#pragma unroll
    for (int c = 0; c < 2; ++c) {
      const int bq = 2 * wr + c;
      if (bq != ho) {
#pragma unroll
        for (int q = 0; q < 4; ++q) {
          f32x4 v;
#pragma unroll
          for (int e = 0; e < 4; ++e) v[e] = fin[c][4 * q + e];
          __builtin_amdgcn_raw_buffer_store_b128(__builtin_bit_cast(u32x4, v), srsrc, gbase + bq * 65536 + ho * 16384 + q * 1024, 0, 16);
        }
      }
    }
    asm volatile("s_waitcnt vmcnt(0)" ::: "memory"); asm volatile("s_barrier" ::: "memory"); SB();
    if (tid == 0) __hip_atomic_store(sy3 + (1 + ho) * 16, (ep3 + 1), RLX_AGENT);
    if (wr == (ho >> 1)) {
      poll3(sy3 + 16, (ep3 + 1), lane, ho);
      if constexpr (USE_ACQ) { __builtin_amdgcn_fence(__ATOMIC_ACQUIRE, "agent"); asm volatile("s_waitcnt vmcnt(0)" ::: "memory"); }
      if (ho == 0 && tid == 0) __hip_atomic_store(sy3, (ep3 + 1), RLX_AGENT);
      f32x16 s = (ho & 1) ? fin[1] : fin[0];
      u32x4 ld[3][4];
#pragma unroll
      for (int i = 0; i < 3; ++i) {
        const int src = (ho + 1 + i) & 3;
#pragma unroll
        for (int q = 0; q < 4; ++q) ld[i][q] = __builtin_amdgcn_raw_buffer_load_b128(srsrc, gbase + ho * 65536 + src * 16384 + q * 1024, 0, 16);
      }
#pragma unroll
      for (int i = 0; i < 3; ++i)
#pragma unroll
        for (int q = 0; q < 4; ++q) {
          const f32x4 v = __builtin_bit_cast(f32x4, ld[i][q]);
#pragma unroll
          for (int e = 0; e < 4; ++e) s[4 * q + e] += v[e];
        }
      u16* dst = out + (size_t)(rg * 128 + ho * 32 + r32) * 1024 + br * 512 + hp * 128 + wn * 64 + kh * 32 + 8 * h;
      *(u32x4*)(dst) = pack8(s, 0);
      *(u32x4*)(dst + 16) = pack8(s, 1);
    }
  }
}

extern "C" void kernel_launch(void* const* d_in, const int* in_sizes, int n_in, void* d_out, int out_size, void* d_ws, size_t ws_size, hipStream_t stream) {
  (void)hipFuncSetAttribute((const void*)fused, hipFuncAttributeMaxDynamicSharedMemorySize, LDS_BYTES);
  fused<<<256, 512, LDS_BYTES, stream>>>((const u16*)d_in[0], (const u16*)d_in[1], (const u16*)d_in[2], (const u16*)d_in[3], (const u16*)d_in[4],
                                         (const u16*)d_in[5], (const u16*)d_in[6], (const u16*)d_in[7], (const u16*)d_in[8], (const u16*)d_in[9],
                                         (char*)d_ws, (u16*)d_out);
}
```
